```python
import torch

def _softmax(x, axis=-1):
    return torch.softmax(x, dim=axis)
def _gelu(x):
    return torch.nn.functional.gelu(x, approximate="tanh")
def _silu(x):
    return torch.nn.functional.silu(x)
def _sigmoid(x):
    return torch.sigmoid(x)
def _layernorm(x, axis=-1, eps=1e-5):
    m = x.mean(dim=axis, keepdim=True)
    v = x.var(dim=axis, keepdim=True, unbiased=False)
    return (x - m) / torch.sqrt(v + eps)
def _scatter_add(tgt, idx, src, axis=0):
    return tgt.clone().index_add_(axis, idx, src)
def _segment_sum(src, seg, n):
    out = torch.zeros((n,) + src.shape[1:], dtype=src.dtype)
    return out.index_add_(0, seg, src)
def _pad(x, pw):
    flat = tuple(v for pair in reversed(pw) for v in pair)
    return torch.nn.functional.pad(x, flat)

kernel_name = 'parallel_sum__ssm_conv_h3__dense_activation_gelu__prompt'

def setup_inputs():

    _w = (lambda a: (lambda t: t.to(torch.bfloat16) if t.is_floating_point() else t)(torch.as_tensor(a)))
    def _A_setup():
        B, S, D = 1, 2048, 576
        ks = 4
        return {
            "q": _w((lambda *s: torch.randn(*s, dtype=torch.bfloat16) * 0.05)(B, S, D)),
            "k": _w((lambda *s: torch.randn(*s, dtype=torch.bfloat16) * 0.2)(B, S, D)),
            "v": _w((lambda *s: torch.randn(*s, dtype=torch.bfloat16) * 0.2)(B, S, D)),
            "shift_k": _w((lambda *s: torch.randn(*s, dtype=torch.bfloat16) * 0.2)(D, ks) * 0.5),
            "ssm_k": _w((lambda *s: torch.randn(*s, dtype=torch.bfloat16) * 0.8)(D, S) * (1.0 / S ** 0.5)),
            "ssm_D": _w((lambda *s: torch.randn(*s, dtype=torch.bfloat16) * 0.2)(D)),
        }
    _A_in = _A_setup()

    def _B_setup():
        B, S, D, F = 1, 2048, 576, 4608
        return {
            "x": _w((lambda *s: torch.randn(*s, dtype=torch.bfloat16) * 0.05)(B, S, D)),
            "w1": _w((lambda *s: torch.randn(*s, dtype=torch.bfloat16) * 0.4)(D, F) * (D ** -0.5)),
            "b1": _w((lambda *s: torch.randn(*s, dtype=torch.bfloat16) * 12.8)(F) * 0.01),
            "w2": _w((lambda *s: torch.randn(*s, dtype=torch.bfloat16) * 0.2)(F, D) * (F ** -0.5)),
            "b2": _w((lambda *s: torch.randn(*s, dtype=torch.bfloat16) * 1.6)(D) * 0.01),
        }
    _B_in = _B_setup()

    return {
        "A_q": _A_in["q"],
        "A_k": _A_in["k"],
        "A_v": _A_in["v"],
        "A_shift_k": _A_in["shift_k"],
        "A_ssm_k": _A_in["ssm_k"],
        "A_ssm_D": _A_in["ssm_D"],
        "B_x": _B_in["x"],
        "B_w1": _B_in["w1"],
        "B_b1": _B_in["b1"],
        "B_w2": _B_in["w2"],
        "B_b2": _B_in["b2"],
    }

def reference(A_q, A_k, A_v, A_shift_k, A_ssm_k, A_ssm_D, B_x, B_w1, B_b1, B_w2, B_b2):
    def _A_ref(q, k, v, shift_k, ssm_k, ssm_D):
        B, S, D = q.shape
        ks = shift_k.shape[-1]
        k_pad = _pad(k, ((0, 0), (ks - 1, 0), (0, 0)))
        k_shift = torch.zeros_like(k)
        for t in range(ks):
            k_shift = k_shift + k_pad[:, t:t + S, :] * shift_k[:, ks - 1 - t][None, None, :]
        kv = k_shift * v
        L = 2 * S
        kv_pad = _pad(kv, ((0, 0), (0, S), (0, 0)))
        kf_pad = _pad(ssm_k, ((0, 0), (0, S)))
        kv_f = (lambda x, axes=(-2,-1): torch.fft.rfft2(x.float(), dim=axes))(kv_pad[:, None, :, :], axes=(1, 2))
        kf_f = (lambda x, axes=(-2,-1): torch.fft.rfft2(x.float(), dim=axes))(kf_pad[:, None, :], axes=(1, 2))
        kf_f = (lambda x, *d: x.permute(*d))(kf_f, 1, 2, 0)
        y_f = kv_f * kf_f[None, :, :, :]
        y = (lambda x, s, axes=(-2,-1): torch.fft.irfft2(x, s=s, dim=axes).to(torch.bfloat16))(y_f, (1, L), axes=(1, 2))[:, 0, :S, :]
        y = y + kv * ssm_D[None, None, :]
        return q * y

    def _B_ref(x, w1, b1, w2, b2):
        h = torch.einsum("bsd,df->bsf", x, w1) + b1
        h = _gelu(h)
        return torch.einsum("bsf,fd->bsd", h, w2) + b2

    x = A_q
    xn = _layernorm(x, axis=-1)
    return x + _A_ref(xn, A_k, A_v, A_shift_k, A_ssm_k, A_ssm_D) + _B_ref(xn, B_w1, B_b1, B_w2, B_b2)
```

```cpp
#include <hip/hip_runtime.h>
#include <stdint.h>
#include <type_traits>

#if defined(__HIP_DEVICE_COMPILE__)
#pragma clang attribute push(__attribute__((target("no-packed-fp32-ops"))), apply_to = function)
#endif
namespace mk {
constexpr int S_ = 2048, D_ = 576, F_ = 4608;
typedef unsigned short u16;
typedef unsigned long long u64;
typedef __bf16 bf16x8 __attribute__((ext_vector_type(8)));
typedef __bf16 bf16x2 __attribute__((ext_vector_type(2)));
typedef short s16x4 __attribute__((ext_vector_type(4)));
typedef short s16x8 __attribute__((ext_vector_type(8)));
typedef float f32x16 __attribute__((ext_vector_type(16)));
typedef float f32x4 __attribute__((ext_vector_type(4)));
typedef float f32x2 __attribute__((ext_vector_type(2)));
typedef unsigned u32x4 __attribute__((ext_vector_type(4)));
typedef unsigned u32x2 __attribute__((ext_vector_type(2)));
#define LAS __attribute__((address_space(3)))
#define GAS __attribute__((address_space(1)))
#define DI __device__ __forceinline__
typedef __amdgpu_buffer_rsrc_t rsrc_t;

constexpr unsigned FL_STRIDE = 128;
constexpr unsigned WS_EPOCH = 0;
constexpr unsigned WS_ARR   = 1024;
constexpr unsigned WS_PROG  = WS_ARR + 256 * FL_STRIDE;
constexpr unsigned WS_PFL   = WS_PROG + 192 * FL_STRIDE;
constexpr unsigned WS_FA    = WS_PFL + 192 * FL_STRIDE;
constexpr unsigned WS_FB    = WS_FA + 64 * FL_STRIDE;
constexpr unsigned WS_FZ    = WS_FB + 64 * FL_STRIDE;
constexpr unsigned WS_TMO   = WS_FZ + 64 * FL_STRIDE;
constexpr unsigned WS_FLAGS_END = 131072;
constexpr unsigned WS_CS    = WS_FLAGS_END;
constexpr unsigned WS_KVT   = WS_CS + 8 * F_ * 4;
constexpr unsigned WS_ZT    = WS_KVT + D_ * S_ * 2;
constexpr unsigned WS_H     = WS_ZT + D_ * S_ * 2;
constexpr unsigned WS_PART  = WS_H + S_ * F_ * 2;
constexpr unsigned WS_STAMP = WS_PART + 48 * 16 * 12288;
constexpr unsigned WS_TL    = WS_STAMP + 256 * 64 * 8;
constexpr unsigned WS_END   = WS_TL + 256 * 1024 * 8;

struct Params {
  const u16 *x, *k, *v, *shk, *ssk, *ssd, *w1, *b1, *w2, *b2;
  u16* out; char* ws;
};

template <int I> using IC = std::integral_constant<int, I>;
template <int B, int E, class F> DI void sfor(F&& f) { if constexpr (B < E) { f(IC<B>{}); sfor<B + 1, E>(f); } }

DI float asf(unsigned u) { return __builtin_bit_cast(float, u); }
DI float b2f(u16 u) { return asf((unsigned)u << 16); }
DI float blo(unsigned u) { return asf(u << 16); }
DI float bhi(unsigned u) { return asf(u & 0xffff0000u); }
DI unsigned pk2(float a, float b) { f32x2 v = {a, b}; return __builtin_bit_cast(unsigned, __builtin_convertvector(v, bf16x2)); }
DI float rb(float f) { return blo(pk2(f, 0.f)); }
DI rsrc_t mkrsrc(const void* p, unsigned bytes) { return __builtin_amdgcn_make_buffer_rsrc((void*)p, 0, bytes, 0x00020000); }
DI unsigned rfl(unsigned v) { return __builtin_amdgcn_readfirstlane(v); }

DI void dma16(unsigned ldsaddr, unsigned voff, rsrc_t srd, unsigned soff) {
  asm volatile("s_mov_b32 m0, %0\n\ts_nop 0\n\tbuffer_load_dwordx4 %1, %2, %3 offen lds" :: "s"(ldsaddr), "v"(voff), "s"(srd), "s"(soff) : "memory");
}
template <unsigned MI, unsigned SI> DI void dma16i(unsigned mb, unsigned voff, rsrc_t srd, unsigned sb) {
  unsigned tmp;
  asm volatile("s_add_u32 m0, %1, %5\n\ts_add_u32 %0, %4, %6\n\tbuffer_load_dwordx4 %2, %3, %0 offen lds"
               : "=&s"(tmp) : "s"(mb), "v"(voff), "s"(srd), "s"(sb), "i"(MI), "i"(SI) : "scc", "memory");
}
template <unsigned SI> DI void ld16i_sc1(u32x4& dst, unsigned voff, rsrc_t srd, unsigned sb) {
  unsigned tmp;
  asm volatile("s_add_u32 %1, %4, %5\n\tbuffer_load_dwordx4 %0, %2, %3, %1 offen sc1" : "=v"(dst), "=&s"(tmp) : "v"(voff), "s"(srd), "s"(sb), "i"(SI) : "scc", "memory");
}
template <int WHICH> DI void dma16_if(unsigned sel, unsigned ldsaddr, unsigned voff, rsrc_t srd, unsigned soff) {
  asm volatile("s_cmp_lg_u32 %4, %5\n\ts_cbranch_scc1 .Ldma%=\n\ts_mov_b32 m0, %0\n\ts_nop 0\n\tbuffer_load_dwordx4 %1, %2, %3 offen lds\n.Ldma%=:"
               :: "s"(ldsaddr), "v"(voff), "s"(srd), "s"(soff), "s"(sel), "i"(WHICH) : "scc", "memory");
}
DI void ld16_sc1(u32x4& dst, unsigned voff, rsrc_t srd, unsigned soff) {
  asm volatile("buffer_load_dwordx4 %0, %1, %2, %3 offen sc1" : "=v"(dst) : "v"(voff), "s"(srd), "s"(soff) : "memory");
}
DI void ld16_sc1_s(u32x4& dst, unsigned voff, rsrc_t srd, unsigned soff) {
  asm volatile("s_nop 4\n\tbuffer_load_dwordx4 %0, %1, %2, %3 offen sc1" : "=v"(dst) : "v"(voff), "s"(srd), "s"(rfl(soff)) : "memory");
}
DI void ld8_sc1_s(u32x2& dst, unsigned voff, rsrc_t srd, unsigned soff) {
  asm volatile("s_nop 4\n\tbuffer_load_dwordx2 %0, %1, %2, %3 offen sc1" : "=v"(dst) : "v"(voff), "s"(srd), "s"(rfl(soff)) : "memory");
}
DI void ld8_sc1(u32x2& dst, unsigned voff, rsrc_t srd, unsigned soff) {
  asm volatile("buffer_load_dwordx2 %0, %1, %2, %3 offen sc1" : "=v"(dst) : "v"(voff), "s"(srd), "s"(soff) : "memory");
}
DI void ld4_sc1(unsigned& dst, unsigned voff, rsrc_t srd, unsigned soff) {
  asm volatile("buffer_load_dword %0, %1, %2, %3 offen sc1" : "=v"(dst) : "v"(voff), "s"(srd), "s"(soff) : "memory");
}
template <int N> DI void waitv() { asm volatile("s_waitcnt vmcnt(%0)" :: "i"(N) : "memory"); }
DI void bar() { __builtin_amdgcn_sched_barrier(0); asm volatile("s_barrier" ::: "memory"); __builtin_amdgcn_sched_barrier(0); }
DI void lgkm0() { asm volatile("s_waitcnt lgkmcnt(0)" ::: "memory"); }

DI int flane() { int l; asm volatile("v_mbcnt_lo_u32_b32 %0, -1, 0\n\tv_mbcnt_hi_u32_b32 %0, -1, %0" : "=v"(l)); return l; }
DI bool wall(bool c) { return __builtin_amdgcn_ballot_w64(!c) == 0; }
DI bool flag_ok(u32x2 v, u64 base, unsigned need) { u64 d = (((u64)v.y << 32) | v.x) - base; return d >= need && d <= 15; }
DI void flag_store(char* ws, unsigned off, u64 val) { __hip_atomic_store((GAS u64*)(ws + off), val, __ATOMIC_RELAXED, __HIP_MEMORY_SCOPE_AGENT); }
DI void spin_flags(char* ws, rsrc_t wsr, unsigned voff, bool active, u64 base, unsigned need) {
  const unsigned tag = (unsigned)base | 1u;
  for (unsigned it = 0;; ++it) {
    u32x2 v; ld8_sc1(v, voff, wsr, 0); asm volatile("s_waitcnt vmcnt(0)" : "+v"(v) :: "memory");
    if (wall(!active || flag_ok(v, base, need))) return;
    if (it > 30000u) { __hip_atomic_store((GAS unsigned*)(ws + WS_TMO), tag, __ATOMIC_RELAXED, __HIP_MEMORY_SCOPE_AGENT); return; }
    if ((it & 255u) == 255u && rfl(__hip_atomic_load((GAS unsigned*)(ws + WS_TMO), __ATOMIC_RELAXED, __HIP_MEMORY_SCOPE_AGENT)) == tag) return;
    __builtin_amdgcn_s_sleep(1);
  }
}
DI void sbar() { asm volatile("s_waitcnt lgkmcnt(0)\n\ts_barrier" ::: "memory"); }
DI void publish_flag(char* ws, unsigned off, u64 val) {
  asm volatile("s_waitcnt vmcnt(0) lgkmcnt(0)\n\ts_barrier" ::: "memory");
  if (threadIdx.x == 0) flag_store(ws, off, val);
}
DI u64 take_epoch(char* ws, const u32x2& epv) {
  unsigned lo, hi;
  asm volatile("v_readfirstlane_b32 %0, %2\n\tv_readfirstlane_b32 %1, %3\n\ts_nop 3" : "=s"(lo), "=s"(hi) : "v"(epv.x), "v"(epv.y) : "memory");
  const u64 base = ((((u64)hi << 32) | lo) + 1) << 4;
  if (threadIdx.x == 0) flag_store(ws, WS_ARR + blockIdx.x * FL_STRIDE, base);
  return base;
}
DI float wave_xor_add8(float v) {
  v += __builtin_bit_cast(float, __builtin_amdgcn_update_dpp(0, __builtin_bit_cast(int, v), 0xB1, 0xf, 0xf, true));
  v += __builtin_bit_cast(float, __builtin_amdgcn_update_dpp(0, __builtin_bit_cast(int, v), 0x4E, 0xf, 0xf, true));
  v += __builtin_bit_cast(float, __builtin_amdgcn_update_dpp(0, __builtin_bit_cast(int, v), 0x141, 0xf, 0xf, true));
  return v;
}

constexpr unsigned R1 = 0;
constexpr unsigned R2 = 9 * 8192;
constexpr unsigned L_MISC = R2 + 73728;
constexpr unsigned L_AUG = L_MISC;
constexpr unsigned L_B1RAW = L_AUG + 6144;
constexpr unsigned L_B2RAW = L_B1RAW + 1024;
constexpr unsigned L_HO = L_B2RAW + 1024;
constexpr unsigned LDS_BYTES = 163840;

#define STAMP(n) do {} while (0)

#define TLK(T, K) do {} while (0)
#define TL(T) do {} while (0)
#define TLDUMP() do {} while (0)
namespace sch {
constexpr int NB = 18;
constexpr int kSel[NB]    = {    0,  0,  1,  2,  0,  1,  2,  0,  1,  0,  2,  0,  1,  2,  1,  2,  1,  2 };
constexpr int kUnit[NB]   = {    1,  0,  1,  1,  2,  0,  0,  3,  2,  4,  2,  5,  3,  3,  4,  4,  5,  5 };
constexpr int kStart[NB]  = {   12, 20, 23, 26, 29, 32, 35, 38, 41, 44, 47, 50, 53, 56, 59, 62, 65, 68 };
constexpr int kRing[NB]   = {   -1, -1,  0,  1, -1,  2,  0, -1,  1, -1,  2, -1,  0,  1,  2,  0,  1,  2 };
constexpr int kHIssue[NB] = {   -1, -1, 20, 21, -1, 27, 29, -1, 36, -1, 41, -1, 47, 50, 54, 56, 59, 62 };
constexpr int kW2Buf[NB]  = {    3,  4,  5,  3,  4,  5,  3,  4,  5,  3,  4,  0,  1,  2,  5,  3,  4,  0 };
constexpr int bsel(int b) { return kSel[b]; }
constexpr int bunit(int b) { return kUnit[b]; }
constexpr int bstart(int b) { return kStart[b]; }
constexpr int NST = bstart(NB - 1) + 3;
constexpr int blk_of(int T) { for (int b = 0; b < NB; ++b) if (T >= bstart(b) && T < bstart(b) + 3) return b; return -1; }
constexpr int gelu0(int u) { return u == 0 ? 9 : u == 5 ? 45 : 9 * u; }
constexpr int chunkW(int u, int k) { return u == 5 ? 46 + k : gelu0(u) + 2 + 2 * k; }
constexpr int chunkR(int u, int k) { return (u == 0 && k == 0) ? 13 : chunkW(u, k) + 1; }
constexpr bool ho_written(int T) { for (int u = 0; u < 6; ++u) if (u != 1) for (int k = 0; k < 4; ++k) if (chunkW(u, k) == T) return true; return false; }
constexpr int cr_unit(int T) { for (int u = 0; u < 6; ++u) if (u != 1) for (int k = 0; k < 4; ++k) if (chunkR(u, k) == T) return u; return -1; }
constexpr int cr_k(int T) { for (int u = 0; u < 6; ++u) if (u != 1) for (int k = 0; k < 4; ++k) if (chunkR(u, k) == T) return k; return -1; }
constexpr int pubK(int T) { return T == 16 ? 1 : T == 23 ? 2 : T == 32 ? 3 : T == 41 ? 4 : T == 50 ? 5 : T == 55 ? 6 : 0; }
constexpr int c1_unit(int c) { return c < 18 ? (c & 1) : 2 + (c - 18) / 9; }
constexpr int c1_kc(int c) { return c < 18 ? (c >> 1) : (c - 18) % 9; }
#define W1LEAD 6
constexpr int c1_issue(int c) { return c < 9 ? -1 : c < 25 ? (c - 9) / 2 + 1 : c - 9 - W1LEAD < 11 ? 11 : c - 9 - W1LEAD; }
constexpr int c1_use(int c) { return c < 18 ? (c >> 1) : c - 9; }
constexpr int x_issue(int kc) { return kc < 4 ? -1 : kc - 3; }
constexpr int x_pos(int kc) { return (kc + 3) & 3; }
constexpr int w2_issue(int b) { return b == 0 ? 7 : b <= 10 ? bstart(b) - 6 : bstart(b) - 9; }
constexpr int w2_buf(int b) { return kW2Buf[b]; }
constexpr int h_issue(int b) { return kHIssue[b]; }
constexpr bool w1_by_C(int T) { return !((T >= 12 && T <= 14) || T >= 20); }
constexpr bool w2_by_C(int T) { return T < 45; }
constexpr int nD(int T, bool C) {
  int n = 0;
  for (int c = 9; c < 54; ++c) if (c1_issue(c) == T) n += T < 9 ? 1 : (w1_by_C(T) == C) ? 2 : 0;
  for (int kc = 4; kc < 9; ++kc) if (x_issue(kc) == T) n += 2;
  for (int b = 0; b < NB; ++b) if (T >= w2_issue(b) && T < w2_issue(b) + 3) n += b == 0 ? 1 : (w2_by_C(T) == C) ? 2 : 0;
  return n;
}
#define HSPREAD 1
constexpr int nH(int T) { int n = 0; for (int b = 2; b < NB; ++b) { if (h_issue(b) < 0) continue; if (HSPREAD) { if (h_issue(b) == T || h_issue(b) + 1 == T) n += 2; } else if (h_issue(b) == T) n += 4; } return n; }
constexpr int cnt(int T, bool C) { return nD(T, C) + (C ? nH(T) : 0); }
constexpr int after(int t0, int T, bool C) { int n = 0; for (int t = t0 + 1; t < T; ++t) n += cnt(t, C); return n; }
constexpr int req(int T, bool C) {
  int r = -2;
  auto mx = [&r](int v) { r = r > v ? r : v; };
  for (int tt = T; tt <= T + 1; ++tt) {
    if (tt == T + 1 && T < 9) break;
    if (tt < 9) { mx(c1_issue(2 * tt)); mx(c1_issue(2 * tt + 1)); mx(x_issue(tt)); }
    else {
      if (tt < 45) { const int t = c1_issue(tt + 9); if (t < 9 || w1_by_C(t) == C) mx(t); }
      const int b = blk_of(tt);
      if (b >= 0) for (int k = 0; k < 3; ++k) { const int t = w2_issue(b) + k; if (b == 0 || w2_by_C(t) == C) mx(t); }
    }
  }
  if (C) { const int b = blk_of(T); if (b >= 2 && bstart(b) == T && h_issue(b) >= 0) mx(h_issue(b) + (HSPREAD ? 1 : 0)); }
  return r;
}
static_assert(cnt(0, true) == 0 && cnt(0, false) == 0 && cnt(1, true) == 4 && cnt(1, false) == 4, "nothing is issued in stage 0");
constexpr int c1_first(int T) { for (int c = 9; c < 54; ++c) if (c1_issue(c) == T) return c; return -1; }
constexpr int c1_second(int T) { for (int c = 10; c < 54; ++c) if (c1_issue(c) == T && c1_issue(c - 1) == T) return c; return -1; }
constexpr int x_at(int T) { for (int kc = 4; kc < 9; ++kc) if (x_issue(kc) == T) return kc; return -1; }
constexpr int w2_at(int T, int k = 0) { for (int b = 0; b < NB; ++b) if (T >= w2_issue(b) && T < w2_issue(b) + 3) { if (k == 0) return b; --k; } return -1; }
constexpr int h_at(int T) { for (int b = 2; b < NB; ++b) if (h_issue(b) == T) return b; return -1; }
constexpr int chk_at(int T) { const int b = h_at(T); return (b >= 2 && bsel(b) == 1) ? b : -1; }
constexpr int val_at(int G, int G0, int RN, int RD, int k) { for (int v = 0; v < 32; ++v) if (G0 + v * RN / RD == G) { if (k == 0) return v; --k; } return -1; }
constexpr int PRO = 9 + 8;
constexpr int waitn(int T, bool C) {
  const int r = req(T, C);
  if (r == -2) return 63;
  if (r >= 0) return after(r, T, C);
  const int need = T < 4 ? 4 * (T + 1) : PRO;
  return PRO - need + after(-1, T, C);
}
}

DI void mlp_role(const Params& p, LAS char* lds, const int i, const int s, const int jn, u32x2 epv) {
  u64 base = 0;
  using namespace sch;
  const int tid = threadIdx.x, lane = tid & 63;
  const int wave = rfl(tid >> 6), rg = wave & 3, role = wave >> 2;
  const int j = 3 * s + jn;
  const int m = lane & 31, hh = lane >> 5, blk = (lane >> 4) & 1, q = (lane & 15) >> 2, pp = lane & 3;
  const rsrc_t rx = mkrsrc(p.x, S_ * D_ * 2), rw1 = mkrsrc(p.w1, D_ * F_ * 2), rw2 = mkrsrc(p.w2, F_ * D_ * 2);
  const rsrc_t rws = mkrsrc(p.ws, WS_END);
  const unsigned ldsb = (unsigned)(size_t)lds;
  STAMP(0);
  const unsigned voX = (8 * wave + (lane >> 3)) * 1152 + (((lane & 7) ^ ((4 * (wave & 1) + (lane >> 4)) & 7)) * 16);
  const unsigned voW1 = (8 * wave + (lane >> 3)) * 9216 + (((lane & 7) ^ (((lane >> 4) & 1) << 2)) * 16);
  unsigned voW2[3];
#pragma unroll
  for (int ii = 0; ii < 3; ++ii) { const unsigned L = 1024 * (3 * wave + ii) + 16 * lane, row = L / 384, ch = (L % 384) / 16; voW2[ii] = row * 1152 + ((ch ^ ((row >> 1) & 1)) * 16); }
  const int jA = 3 * s + (jn + 1) % 3, jB = 3 * s + (jn + 2) % 3;
  const unsigned mW = ldsb + wave * 1024, mW3 = ldsb + wave * 3072, mC2 = ldsb + rg * 2048, mC6 = ldsb + rg * 6144;
  const unsigned sW1 = 768 * j, sX = 128 * i * 1152;
  const unsigned sW2[3] = {(unsigned)(384 * j * 1152 + 384 * jn), (unsigned)(384 * jA * 1152 + 384 * jn), (unsigned)(384 * jB * 1152 + 384 * jn)};
  auto dmaW1 = [&](auto Cc) { constexpr int c = decltype(Cc)::value;
    dma16i<R1 + (c % 9) * 8192, (64 * c1_kc(c)) * 9216 + 128 * c1_unit(c)>(mW, voW1, rw1, sW1); };
  auto dmaX = [&](auto Kc) { constexpr int kc = decltype(Kc)::value;
    dma16i<R2 + x_pos(kc) * 16384, kc * 128>(mW, voX, rx, sX);
    dma16i<R2 + x_pos(kc) * 16384 + 8192, 64 * 1152 + kc * 128>(mW, voX, rx, sX); };
  auto dmaW2 = [&](auto Bc, auto Kc) { constexpr int b = decltype(Bc)::value, k = decltype(Kc)::value;
    dma16i<w2_buf(b) * 24576 + k * 1024, 64 * bunit(b) * 1152>(mW3, voW2[k], rw2, sW2[bsel(b)]); };
  const unsigned voW1c = (16 * rg + (lane >> 3)) * 9216 + (((lane & 7) ^ (((lane >> 4) & 1) << 2)) * 16);
  unsigned voW2c[3];
  auto mkW2c = [&](int l) {
#pragma unroll
    for (int ii = 0; ii < 3; ++ii) { const unsigned L = 1024 * ii + 16 * l, row = L / 384, ch = (L % 384) / 16; voW2c[ii] = (16 * rg + row) * 1152 + ((ch ^ ((row >> 1) & 1)) * 16); }
  };
  auto dmaW1c = [&](auto Cc, auto Hc) { constexpr int c = decltype(Cc)::value, h = decltype(Hc)::value;
    dma16i<R1 + (c % 9) * 8192 + h * 1024, (64 * c1_kc(c) + 8 * h) * 9216 + 128 * c1_unit(c)>(mC2, voW1c, rw1, sW1); };
  auto dmaW2c = [&](auto Bc, auto Kc) { constexpr int b = decltype(Bc)::value, k = decltype(Kc)::value;
    dma16i<w2_buf(b) * 24576 + k * 1024, (64 * bunit(b) + 8 * (k / 3)) * 1152>(mC6, voW2c[k % 3], rw2, sW2[bsel(b)]); };
  auto site = [&](auto Tc, auto Sc, auto Cr) {
    constexpr int T = decltype(Tc)::value, st = decltype(Sc)::value; constexpr bool C = decltype(Cr)::value;
    if constexpr (T < 9) {
      if constexpr (st == 0 && c1_first(T) >= 0) dmaW1(IC<c1_first(T)>{});
      if constexpr (st == 1 && c1_second(T) >= 0) dmaW1(IC<c1_second(T)>{});
      if constexpr (st == 2 && x_at(T) >= 0) dmaX(IC<x_at(T)>{});
    } else if constexpr (w1_by_C(T) == C) {
      if constexpr (st < 2 && c1_first(T) >= 0) dmaW1c(IC<c1_first(T)>{}, Sc);
      if constexpr (st < 2 && c1_second(T) >= 0) dmaW1c(IC<c1_second(T)>{}, Sc);
    }
    if constexpr (w2_at(T) == 0) { if constexpr (st == 2) dmaW2(IC<0>{}, IC<T - w2_issue(0)>{}); }
    else if constexpr (w2_by_C(T) == C && w2_at(T) > 0 && st >= 2) dmaW2c(IC<w2_at(T)>{}, IC<2 * (T - w2_issue(w2_at(T) < 0 ? 0 : w2_at(T))) + (st - 2)>{});
    if constexpr (w2_by_C(T) == C && w2_at(T, 1) > 0 && st < 2) dmaW2c(IC<w2_at(T, 1)>{}, IC<2 * (T - w2_issue(w2_at(T, 1) < 0 ? 0 : w2_at(T, 1))) + st>{});
  };
  auto allsites = [&](auto Tc, auto Cr) { site(Tc, IC<0>{}, Cr); site(Tc, IC<1>{}, Cr); site(Tc, IC<2>{}, Cr); site(Tc, IC<3>{}, Cr); };
  if (wave == 0) dma16(ldsb + L_B1RAW, lane * 16, mkrsrc(p.b1 + 384 * j, 768), 0);
  if (wave == 1) dma16(ldsb + L_B2RAW, lane * 16, mkrsrc(p.b2 + 192 * jn, 384), 0);
  sfor<0, 4>([&](auto Kc) { constexpr int k = decltype(Kc)::value; dmaX(Kc); dmaW1(IC<2 * k>{}); dmaW1(IC<2 * k + 1>{}); });
  dmaW1(IC<8>{});

  const int sw = (q >> 1) & 1;
  const unsigned a1row = ldsb + R1 + (8 * hh + q) * 128 + 32 * blk + 8 * pp;
  const unsigned a1e = a1row + 64 * sw, a1o = a1row + 64 * (1 - sw);
  const unsigned a2row = ldsb + (4 * hh + q) * 384 + 96 * (2 * blk + (pp >> 1)) + 8 * (pp & 1);
  const unsigned a2e = a2row + 16 * sw, a2o = a2row + 16 * (1 - sw);
  const unsigned ax = ldsb + R2 + (32 * rg + m) * 128 + ((hh ^ ((m >> 1) & 7)) * 16);
  auto trr = [&](unsigned addr, unsigned d) -> bf16x8 {
    s16x4 lo = __builtin_amdgcn_ds_read_tr16_b64_v4i16((LAS s16x4*)(size_t)addr);
    s16x4 hi = __builtin_amdgcn_ds_read_tr16_b64_v4i16((LAS s16x4*)(size_t)(addr + d));
    return __builtin_bit_cast(bf16x8, __builtin_shufflevector(lo, hi, 0, 1, 2, 3, 4, 5, 6, 7));
  };
  const f32x16 zero16 = {};
  const unsigned hbase_own = WS_H + (unsigned)(i * 12 + j) * 98304u;
  const unsigned voH = rg * 4096 + lane * 16;
  float sd = 1.f, k0 = 0.f, k1 = 0.f, mean_f = 0.f;
  bf16x8 augB = {};
  u32x2 pollv = {0u, 0u};
  float st1 = 0.f, csl[2] = {0.f, 0.f};

  bf16x8 xr[36];
  f32x16 acc[2][2];
#define PFP_ 4
  constexpr int PF = 4, PFP = PFP_;
  bf16x8 pf[PF];
  unsigned b1e = a1e + role * 8192, b1o = a1o + role * 8192, b1en = 0, b1on = 0;
  asm volatile("" : "+v"(b1e), "+v"(b1o));
  auto rd1 = [&](auto Ic) -> bf16x8 { constexpr int I = decltype(Ic)::value; return trr(((I & 1) ? b1o : b1e) + (I >> 1) * 2048, 512); };
  auto rd1n = [&](auto Dc, auto Ic) -> bf16x8 { constexpr int D = decltype(Dc)::value, I = decltype(Ic)::value;
    if constexpr (D > 0) return trr(((I & 1) ? b1o : b1e) + D * 8192 + (I >> 1) * 2048, 512);
    else return trr(((I & 1) ? b1on : b1en) + (I >> 1) * 2048, 512); };
  auto adv1 = [&](auto Dc) { constexpr int D = decltype(Dc)::value; b1e += D * 8192; b1o += D * 8192; asm volatile("" : "+v"(b1e), "+v"(b1o)); };
  u32x4 hv[4];
  const unsigned aHO = ldsb + L_HO + rg * 1024 + lane * 16;
  float ez[32], eo[32];
  auto epigap = [&](auto Uc, auto Bc, auto Gc, auto G0c, auto RNc, auto RDc) {
    constexpr int uu = decltype(Uc)::value, BUF = decltype(Bc)::value, G = decltype(Gc)::value, G0 = decltype(G0c)::value, RN = decltype(RNc)::value, RD = decltype(RDc)::value;
    sfor<0, 2>([&](auto Kc) { constexpr int v = val_at(G - 2, G0, RN, RD, decltype(Kc)::value); if constexpr (v >= 0) ez[v] = __builtin_fmaf(ez[v], sd, sd); });
    sfor<0, 2>([&](auto Kc) { constexpr int v = val_at(G, G0, RN, RD, decltype(Kc)::value);
      if constexpr (v >= 0) { const float w = acc[BUF][v >> 4][v & 15], w2 = w * w, pz = __builtin_fmaf(k1, w2, k0); ez[v] = pz * w; } });
    sfor<0, 2>([&](auto Kc) { constexpr int v = val_at(G - 2, G0, RN, RD, decltype(Kc)::value); if constexpr (v >= 0) ez[v] = __builtin_amdgcn_rcpf(ez[v]); });
    sfor<0, 2>([&](auto Kc) { constexpr int v = val_at(G - 1, G0, RN, RD, decltype(Kc)::value); if constexpr (v >= 0) ez[v] = __builtin_amdgcn_exp2f(ez[v]); });
    sfor<0, 2>([&](auto Kc) { constexpr int v = val_at(G - 3, G0, RN, RD, decltype(Kc)::value);
      if constexpr (v >= 0) {
        eo[v] = acc[BUF][v >> 4][v & 15] * ez[v];
        if constexpr (v & 1) hv[v >> 3][(v & 7) >> 1] = pk2(eo[v - 1], eo[v]);
        if constexpr ((v & 7) == 7) {
          if constexpr (uu == 1) {
            __builtin_amdgcn_raw_buffer_store_b128(hv[v >> 3], rws, voH + (v >> 3) * 1024, hbase_own + uu * 16384, 16);
          } else {
            *(LAS u32x4*)(size_t)(aHO + ((v >> 3) & 1) * 4096) = hv[v >> 3];
          }
        }
      } });
  };
  auto aug = [&](auto Uc, auto Bc) {
    constexpr int uu = decltype(Uc)::value, BUF = decltype(Bc)::value;
#pragma unroll
    for (int t = 0; t < 2; ++t) {
      const bf16x8 af = *(LAS bf16x8*)(lds + L_AUG + (64 * uu + 32 * t + m) * 16);
      acc[BUF][t] = __builtin_amdgcn_mfma_f32_32x32x16_bf16(af, augB, acc[BUF][t], 0, 0, 0);
    }
  };
  auto g1stage = [&](auto Tc, auto Cc, auto Bc, auto Nc, auto Cr, auto&& work, auto&& syncnext) {
    constexpr int T = decltype(Tc)::value, c = decltype(Cc)::value, BUF = decltype(Bc)::value, NEXT = decltype(Nc)::value, kc = c1_kc(c);
    constexpr int PF = T < 9 ? 4 : PFP, G0 = T < 9 ? 0 : (8 * (T - 9)) % PF;
    constexpr int DN = NEXT >= 0 ? NEXT % 9 - c % 9 : 1;
    if constexpr (NEXT >= 0 && DN < 0) { b1en = b1e + DN * 8192; b1on = b1o + DN * 8192; asm volatile("" : "+v"(b1en), "+v"(b1on)); }
    __builtin_amdgcn_sched_barrier(0);
    sfor<0, 8>([&](auto Ic) {
      constexpr int I = decltype(Ic)::value;
      const bf16x8 cur = pf[(G0 + I) % PF];
      if constexpr (I + PF < 8) pf[(G0 + I) % PF] = rd1(IC<I + PF>{});
      else if constexpr (NEXT >= 0) pf[(G0 + I) % PF] = rd1n(IC<DN>{}, IC<I + PF - 8>{});
      acc[BUF][I & 1] = __builtin_amdgcn_mfma_f32_32x32x16_bf16(cur, xr[4 * kc + (I >> 1)], (kc == 0 && I < 2) ? zero16 : acc[BUF][I & 1], 0, 0, 0);
      __builtin_amdgcn_sched_barrier(0);
      if constexpr (I == 7 && NEXT >= 0) adv1(IC<DN>{});
      if constexpr (I == 1) site(Tc, IC<0>{}, Cr);
      if constexpr (I == 3) site(Tc, IC<1>{}, Cr);
      if constexpr (I == 5) site(Tc, IC<2>{}, Cr);
      if constexpr (I == 7) { asm volatile("" : "+v"(acc[BUF][0]), "+v"(acc[BUF][1])); syncnext(); }
      work(Ic, cur);
      __builtin_amdgcn_sched_barrier(0);
    });
  };
  auto dot2 = [](unsigned a, unsigned b, float c) { return __builtin_amdgcn_fdot2_f32_bf16(__builtin_bit_cast(bf16x2, a), __builtin_bit_cast(bf16x2, b), c, false); };
  auto xfinish = [&]() {
    auto both = [](float v) { const unsigned u = __builtin_bit_cast(unsigned, v); const auto r = __builtin_amdgcn_permlane32_swap(u, u, false, false); return asf(r[0]) + asf(r[1]); };
    const float t1 = both(st1), c0 = both(csl[0]), c1 = both(csl[1]);
    if (hh == 0) {
      *(LAS float*)(lds + L_HO + role * 512 + (32 * rg + m) * 4) = t1;
      if (rg == 0) {
#pragma unroll
        for (int t = 0; t < 2; ++t) { const float cs = t ? c1 : c0, ch = rb(cs); const int f = 64 * role + 32 * t + m;
          *(LAS u32x4*)(lds + L_AUG + f * 16) = u32x4{pk2(ch, cs - ch), *(LAS u16*)(lds + L_B1RAW + f * 2), 0u, 0u}; }
      }
    }
    lgkm0();
  };
  auto takestats = [&]() {
    const float s1 = *(LAS float*)(lds + L_HO + (32 * rg + m) * 4), s2 = *(LAS float*)(lds + L_HO + 512 + (32 * rg + m) * 4);
    const float mean = s1 * (1.0f / D_), var = __builtin_fmaxf(__builtin_fmaf(-mean, mean, s2 * (1.0f / D_)), 0.f);
    mean_f = rb(mean); sd = rb(__builtin_sqrtf(rb(rb(var) + 1e-5f)));
    const float r = 1.0f / sd; k0 = -2.302208198f * r; k1 = -0.1029432397f * r * r * r;
    const u32x4 bb = {hh ? 0u : (pk2(-mean_f, -mean_f)), hh ? 0u : (pk2(sd, 0.f)), 0u, 0u};
    augB = __builtin_bit_cast(bf16x8, bb);
  };
  auto syncX = [&](auto Tc, auto Cr) {
    constexpr int T = decltype(Tc)::value; constexpr bool C = decltype(Cr)::value;
    waitv<waitn(T, C)>();
    bar();
    if constexpr (T == 0) { base = take_epoch(p.ws, epv); STAMP(1); }
  };
  auto xphase = [&](auto Cr, auto&& sync9) {
    constexpr bool C = decltype(Cr)::value;
    syncX(IC<0>{}, Cr);
    sfor<0, 9>([&](auto Tc) {
      constexpr int T = decltype(Tc)::value;
      constexpr int cc = 2 * T + (C ? 1 : 0);
      if constexpr (T > 0) adv1(IC<cc % 9 - (cc - 2) % 9>{});
      sfor<0, 4>([&](auto Kc) { constexpr int ks = decltype(Kc)::value;
        xr[4 * T + ks] = *(LAS bf16x8*)(size_t)((ax ^ (ks << 5)) + x_pos(T) * 16384); });
      sfor<0, PF>([&](auto Ic) { pf[decltype(Ic)::value] = rd1(Ic); });
      g1stage(Tc, IC<2 * T + (C ? 1 : 0)>{}, IC<(C ? 1 : 0)>{}, IC<-1>{}, Cr, [&](auto Ic, const bf16x8& cur) {
        constexpr int I = decltype(Ic)::value;
        const u32x4 a = __builtin_bit_cast(u32x4, cur);
#pragma unroll
        for (int e = 0; e < 4; ++e) csl[I & 1] = dot2(a[e], 0x3f803f80u, csl[I & 1]);
        if constexpr ((I & 1) == 0) { const u32x4 x = __builtin_bit_cast(u32x4, xr[4 * T + (I >> 1)]);
#pragma unroll
          for (int e = 0; e < 4; ++e) st1 = dot2(x[e], C ? x[e] : 0x3f803f80u, st1); }
      }, [&]() { if constexpr (T < 8) syncX(IC<T + 1>{}, Cr); else sync9(); });
    });
  };
  if (role == 0) {
    auto syncP = [&](auto Tc) {
      constexpr int T = decltype(Tc)::value;
      if constexpr (T == 9) xfinish();
      waitv<waitn(T, false)>();
      if constexpr (T > 45 && T <= 50) lgkm0();
      if constexpr (T - 1 < 45 && ho_written(T - 1)) asm volatile("s_waitcnt lgkmcnt(6)" ::: "memory");
      bar();
      if constexpr (T == 9) takestats();
    };
    xphase(std::false_type{}, [&]() { syncP(IC<9>{}); });
    STAMP(2);
    sfor<9, NST>([&](auto Tc) {
      constexpr int T = decltype(Tc)::value;
      if constexpr (T < 45) {
        constexpr int u = 2 + (T - 9) / 9, kc = (T - 9) % 9, PU = u == 2 ? 0 : u - 1;
        constexpr int UB = (u + 1) & 1, EB = UB ^ 1;
        if constexpr (T == 9) { adv1(IC<18 % 9 - 16 % 9>{}); sfor<0, PFP>([&](auto Ic) { pf[decltype(Ic)::value] = rd1(Ic); }); }
        if constexpr (kc == 0) aug(IC<PU>{}, IC<EB>{});
        g1stage(Tc, IC<T + 9>{}, IC<UB>{}, IC<(T + 1 < 45 ? T + 10 : -1)>{}, std::false_type{}, [&](auto Ic, const bf16x8&) {
          epigap(IC<PU>{}, IC<EB>{}, IC<8 * kc + decltype(Ic)::value>{}, IC<3>{}, IC<2>{}, IC<1>{}); }, [&]() { syncP(IC<T + 1>{}); });
      } else {
        if constexpr (T == 45) { STAMP(3); aug(IC<5>{}, IC<0>{}); mkW2c(flane()); }
        allsites(Tc, std::false_type{});
        if constexpr (T < 50) sfor<0, 8>([&](auto Ic) { epigap(IC<5>{}, IC<0>{}, IC<8 * (T - 45) + decltype(Ic)::value>{}, IC<3>{}, IC<1>{}, IC<1>{}); });
        if constexpr (T + 1 < NST) syncP(IC<T + 1>{});
      }
    });
    TLDUMP();
    waitv<0>(); bar();
    if (wave < 3) {
      const int l = flane();
#pragma unroll
      for (int ii = 0; ii < 4; ++ii) { const int pc = wave * 4 + ii;
        dma16(ldsb + pc * 1024, (4 * pc + (l >> 4)) * 4096 + (l & 15) * 16, rws, WS_ZT + (192 * jn + 48 * s) * 4096 + (128 * i) * 2); }
    }
    waitv<0>(); bar();
    bar();
    return;
  }
  f32x16 acc2[6];
  u32x4 hring[3][4], hown[4];
  unsigned b2e[2], b2o[2];
  auto base2 = [&](auto Bc) { constexpr int b = decltype(Bc)::value;
    unsigned e = a2e + w2_buf(b) * 24576, o = a2o + w2_buf(b) * 24576; asm volatile("" : "+v"(e), "+v"(o)); b2e[b & 1] = e; b2o[b & 1] = o; };
  auto rd2 = [&](auto Bc, auto Gc) -> bf16x8 { constexpr int b = decltype(Bc)::value, G = decltype(Gc)::value, ks = G / 6, tn = G % 6;
    return trr(((tn & 1) ? b2o[b & 1] : b2e[b & 1]) + ks * 16 * 384 + 32 * (tn >> 1), 8 * 384); };
  const unsigned hbA = WS_H + (unsigned)(i * 12 + jA) * 98304u, hbB = WS_H + (unsigned)(i * 12 + jB) * 98304u;
  const unsigned sH[3] = {hbase_own, hbA, hbB};
  auto issueH = [&](auto Tc, auto Gc) {
    constexpr int T = decltype(Tc)::value, G = decltype(Gc)::value;
    sfor<0, 2>([&](auto Hc) {
      constexpr int half = decltype(Hc)::value, b = h_at(HSPREAD ? T - half : T);
      if constexpr (b >= 2) {
        constexpr int r = kRing[b], k = 2 * half + G;
        ld16i_sc1<bunit(b) * 16384 + k * 1024>(hring[r][k], voH, rws, sH[bsel(b)]);
      }
    });
  };
  u32x2 progv = {0u, 0u};
  unsigned proga = WS_PROG + (i * 12 + j) * FL_STRIDE;
  auto voPR = [&](int l) -> unsigned { return WS_PROG + (i * 12 + (l == 0 ? jA : jB)) * FL_STRIDE; };
  auto actFZ = [&](int l) { return ((192 * jn + 48 * s) / 9 + l) <= (192 * jn + 48 * s + 47) / 9; };
  auto voFZ = [&](int l) -> unsigned { return actFZ(l) ? WS_FZ + ((192 * jn + 48 * s) / 9 + l) * FL_STRIDE : WS_FZ; };
  u32x2 pollz = {0u, 0u};
  unsigned csv[8];
  auto actFA = [&](int l) { return l < 16 && (384 * j + 128) / 576 + (l >> 3) <= (384 * j + 383) / 576; };
  auto voFA = [&](int l) -> unsigned { return actFA(l) ? WS_FA + (((384 * j + 128) / 576 + (l >> 3)) * 8 + (l & 7)) * FL_STRIDE : WS_FA; };
  constexpr auto needOf = [](int b) { return sch::bunit(b) == 1 ? 1 : sch::bunit(b) == 0 ? 2 : sch::bunit(b) + 1; };
  auto syncC = [&](auto Tc) {
    constexpr int T = decltype(Tc)::value, b = blk_of(T);
    if constexpr (T == 9) xfinish();
    if constexpr (T == 15) {
      if (wave == 4) { waitv<after(12, 15, true)>(); asm volatile("" : "+v"(pollv)); const int l = flane(); if (!wall(!actFA(l) || flag_ok(pollv, base, 1))) spin_flags(p.ws, rws, voFA(l), actFA(l), base, 1); }
    }
    if constexpr (T == 17) {
      asm volatile("s_waitcnt vmcnt(%8)" : "+v"(csv[0]), "+v"(csv[1]), "+v"(csv[2]), "+v"(csv[3]), "+v"(csv[4]), "+v"(csv[5]), "+v"(csv[6]), "+v"(csv[7]) : "i"(after(14, 17, true)) : "memory");
      const float cs = ((asf(csv[0]) + asf(csv[1])) + (asf(csv[2]) + asf(csv[3]))) + ((asf(csv[4]) + asf(csv[5])) + (asf(csv[6]) + asf(csv[7]))), ch = rb(cs);
      const int f = 128 + 64 * rg + flane();
      *(LAS u32x4*)(lds + L_AUG + f * 16) = u32x4{pk2(ch, cs - ch), *(LAS u16*)(lds + L_B1RAW + f * 2), 0u, 0u};
      lgkm0();
    }
    if constexpr (chk_at(T) >= 0) {
      constexpr int bb = chk_at(T);
      if (wave == 4) { waitv<after(T - 3, T, true)>(); asm volatile("" : "+v"(pollv)); const int l = flane();
        if (!wall(l >= 2 || flag_ok(pollv, base, needOf(bb)))) spin_flags(p.ws, rws, voPR(l), l < 2, base, needOf(bb));
        if constexpr (bb == NB - 2) { asm volatile("" : "+v"(pollz)); if (!wall(!actFZ(l) || flag_ok(pollz, base, 1))) spin_flags(p.ws, rws, voFZ(l), actFZ(l), base, 1); } }
    }
    if constexpr (pubK(T) > 0) waitv<after(T - 5, T, true)>();
    waitv<waitn(T, true)>();
    if constexpr (b >= 2 && bstart(b < 0 ? 0 : b) == T && kRing[b < 0 ? 0 : b] >= 0) { constexpr int r = kRing[b < 0 ? 0 : b]; asm volatile("" : "+v"(hring[r][0]), "+v"(hring[r][1]), "+v"(hring[r][2]), "+v"(hring[r][3])); }
    bar();
    if constexpr (pubK(T) > 0) {
      progv.x += 1;
      if (wave == 4) asm volatile("s_mov_b64 exec, 1\n\tbuffer_store_dwordx2 %0, %1, %2, 0 offen sc1\n\ts_mov_b64 exec, -1" :: "v"(progv), "v"(proga), "s"(rws) : "memory");
    }
    if constexpr (T == 9) takestats();
    if constexpr (T == 13) { if (wave == 4) ld8_sc1(pollv, voFA(flane()), rws, 0); }
    if constexpr (T == 15) {
      const unsigned vo = (384 * j + 128 + 64 * rg + flane()) * 4;
#pragma unroll
      for (int r = 0; r < 8; ++r) ld4_sc1(csv[r], vo, rws, WS_CS + r * F_ * 4);
    }
    if constexpr (chk_at(T + 2) >= 0) {
      if (wave == 4) { const int l = flane(); ld8_sc1(pollv, voPR(l), rws, 0); if constexpr (chk_at(T + 2) == NB - 2) ld8_sc1(pollz, voFZ(l), rws, 0); }
    }
    constexpr int cu = cr_unit(T), ck = cr_k(T);
    if constexpr (cu >= 0) hown[ck] = *(LAS u32x4*)(size_t)(aHO + (ck & 1) * 4096);
  };
  auto chunk_store = [&](auto Tc) {
    constexpr int T = decltype(Tc)::value, cu = cr_unit(T), ck = cr_k(T);
    if constexpr (cu >= 0) __builtin_amdgcn_raw_buffer_store_b128(hown[ck], rws, voH + ck * 1024, hbase_own + cu * 16384, 16);
  };
  mkW2c(lane);
  xphase(std::true_type{}, [&]() { syncC(IC<9>{}); });
  progv = u32x2{(unsigned)base, (unsigned)(base >> 32)};
  __builtin_amdgcn_s_setprio(1);
  STAMP(2);
  sfor<9, NST>([&](auto Tc) {
    constexpr int T = decltype(Tc)::value, b = blk_of(T), part = b >= 0 ? T - bstart(b) : 0;
    if constexpr (T < 12) {
      if constexpr (T == 9) aug(IC<1>{}, IC<1>{});
      allsites(Tc, std::true_type{});
      sfor<0, 8>([&](auto Ic) { epigap(IC<1>{}, IC<1>{}, IC<8 * (T - 9) + decltype(Ic)::value>{}, IC<1>{}, IC<5>{}, IC<8>{}); });
      if constexpr (T == 11) {
#pragma unroll
        for (int k = 0; k < 4; ++k) hown[k] = hv[k];
        base2(IC<0>{}); sfor<0, PF>([&](auto Ic) { pf[decltype(Ic)::value] = rd2(IC<0>{}, Ic); });
      }
      syncC(IC<T + 1>{});
    } else if constexpr (b < 0) {
      allsites(Tc, std::true_type{});
      issueH(Tc, IC<0>{}); issueH(Tc, IC<1>{});
      chunk_store(Tc);
      if constexpr (blk_of(T + 1) >= 0) { base2(IC<blk_of(T + 1)>{}); sfor<0, PF>([&](auto Ic) { pf[decltype(Ic)::value] = rd2(IC<blk_of(T + 1)>{}, Ic); }); }
      syncC(IC<T + 1>{});
    } else {
      constexpr bool nextblk = part == 2 && blk_of(T + 1) >= 0;
      if constexpr (nextblk) base2(IC<b + 1>{});
      __builtin_amdgcn_sched_barrier(0);
      sfor<0, 8>([&](auto Ic) {
        constexpr int I = decltype(Ic)::value, G = 8 * part + I, ks = G / 6, tn = G % 6;
        const bf16x8 cur = pf[I % PF];
        if constexpr (G + PF < 24) pf[I % PF] = rd2(IC<b>{}, IC<G + PF>{});
        else if constexpr (nextblk) pf[I % PF] = rd2(IC<b + 1>{}, IC<G + PF - 24>{});
        u32x4 hb;
        if constexpr (bsel(b) == 0) hb = hown[ks]; else hb = hring[kRing[b] < 0 ? 0 : kRing[b]][ks];
        acc2[tn] = __builtin_amdgcn_mfma_f32_32x32x16_bf16(cur, __builtin_bit_cast(bf16x8, hb), (b == 0 && ks == 0) ? zero16 : acc2[tn], 0, 0, 0);
        __builtin_amdgcn_sched_barrier(0);
        if constexpr (I == 0) site(Tc, IC<0>{}, std::true_type{});
        if constexpr (I == 2) site(Tc, IC<1>{}, std::true_type{});
        if constexpr (I == 1) issueH(Tc, IC<0>{});
        if constexpr (I == 3) issueH(Tc, IC<1>{});
        if constexpr (I == 4) site(Tc, IC<2>{}, std::true_type{});
        if constexpr (I == 5) chunk_store(Tc);
        if constexpr (I == 6) site(Tc, IC<3>{}, std::true_type{});
        if constexpr (I == 7) {
          asm volatile("" : "+v"(acc2[0]), "+v"(acc2[1]), "+v"(acc2[2]), "+v"(acc2[3]), "+v"(acc2[4]), "+v"(acc2[5]));
          if constexpr (T + 1 < NST) syncC(IC<T + 1>{});
        }
        __builtin_amdgcn_sched_barrier(0);
      });
    }
  });
  asm volatile("" :: "v"(progv), "v"(proga));
  STAMP(4);
  TLDUMP();
  const int tl = flane(), tm = tl & 31, thh = tl >> 5;
  const int pos = 128 * i + 32 * rg + tm;
  u32x2 xv[6];
#pragma unroll
  for (int tn = 0; tn < 6; ++tn)
    asm volatile("buffer_load_dwordx2 %0, %1, %2, %3 offen" : "=v"(xv[tn]) : "v"(pos * 1152 + (8 * tn + 4 * thh) * 2), "s"(rx), "s"((192 * jn + 48 * s) * 2) : "memory");
  lgkm0(); bar();
  const unsigned pbase = WS_PART + (unsigned)(i * 3 + jn) * (16 * 12288) + rg * 3072;
  float own[24];
  sfor<0, 4>([&](auto Gc) {
    constexpr int g = decltype(Gc)::value;
    if (g != s) {
#pragma unroll
      for (int c = 0; c < 3; ++c) {
        const u32x4 va = {pk2(acc2[2 * c][4 * g], acc2[2 * c][4 * g + 1]), pk2(acc2[2 * c][4 * g + 2], acc2[2 * c][4 * g + 3]),
                          pk2(acc2[2 * c + 1][4 * g], acc2[2 * c + 1][4 * g + 1]), pk2(acc2[2 * c + 1][4 * g + 2], acc2[2 * c + 1][4 * g + 3])};
        __builtin_amdgcn_raw_buffer_store_b128(va, rws, tl * 16 + c * 1024, pbase + (g * 4 + s) * 12288, 16);
      }
    } else {
#pragma unroll
      for (int tn = 0; tn < 6; ++tn)
#pragma unroll
        for (int e = 0; e < 4; ++e) own[4 * tn + e] = acc2[tn][4 * g + e];
    }
  });
  asm volatile("s_waitcnt vmcnt(0)" : "+v"(xv[0]), "+v"(xv[1]), "+v"(xv[2]), "+v"(xv[3]), "+v"(xv[4]), "+v"(xv[5]) :: "memory");
  bar();
  STAMP(5);
  if (wave == 4 && tl == 0) flag_store(p.ws, WS_PFL + ((i * 3 + jn) * 4 + s) * FL_STRIDE, base + 1);
  bar();
  float pre[24];
  {
    const float rstd = 1.0f / sd;
#pragma unroll
    for (int tn = 0; tn < 6; ++tn) {
      const int cl = 8 * tn + 4 * thh;
      const s16x4 zt = __builtin_amdgcn_ds_read_tr16_b64_v4i16((LAS s16x4*)(lds + (cl + ((tl & 15) >> 2)) * 256 + (32 * rg + 16 * ((tl >> 4) & 1) + 4 * (tl & 3)) * 2));
      const u32x2 bv = *(LAS u32x2*)(lds + L_B2RAW + (48 * s + cl) * 2);
#pragma unroll
      for (int e = 0; e < 4; ++e) {
        const float xe = (e & 1) ? bhi(xv[tn][e >> 1]) : blo(xv[tn][e >> 1]);
        const float be_ = (e & 1) ? bhi(bv[e >> 1]) : blo(bv[e >> 1]);
        const float xn = rb((xe - mean_f) * rstd);
        pre[4 * tn + e] = __builtin_fmaf(xn, b2f((u16)zt[e]), xe) + (own[4 * tn + e] + be_);
      }
    }
  }
  spin_flags(p.ws, rws, WS_PFL + ((i * 3 + jn) * 4 + (tl & 3)) * FL_STRIDE, tl < 4 && tl != s, base, 1);
  STAMP(6);
  u32x4 pa[3][3];
#pragma unroll
  for (int o = 0; o < 3; ++o) { const int so = o + (o >= s ? 1 : 0);
#pragma unroll
    for (int c = 0; c < 3; ++c) ld16_sc1_s(pa[o][c], tl * 16 + c * 1024, rws, pbase + (s * 4 + so) * 12288); }
  asm volatile("s_waitcnt vmcnt(0)" : "+v"(pa[0][0]), "+v"(pa[0][1]), "+v"(pa[0][2]), "+v"(pa[1][0]), "+v"(pa[1][1]), "+v"(pa[1][2]), "+v"(pa[2][0]), "+v"(pa[2][1]), "+v"(pa[2][2]) :: "memory");
  STAMP(8);
#pragma unroll
  for (int tn = 0; tn < 6; ++tn) {
    float o[4];
#pragma unroll
    for (int e = 0; e < 4; ++e) {
      float sum = pre[4 * tn + e];
#pragma unroll
      for (int oo = 0; oo < 3; ++oo) { const unsigned wd = pa[oo][tn >> 1][2 * (tn & 1) + (e >> 1)]; sum += (e & 1) ? bhi(wd) : blo(wd); }
      o[e] = sum;
    }
    const u32x2 ov = {pk2(o[0], o[1]), pk2(o[2], o[3])};
    *(GAS u32x2*)(p.out + (size_t)pos * D_ + 192 * jn + 48 * s + 8 * tn + 4 * thh) = ov;
  }
  STAMP(7);
}

constexpr unsigned A_CH = 12928;
constexpr unsigned A_H0 = 32, A_H1 = A_H0 + 4096 + 64, A_X = A_H1 + 4096 + 512  ;
static_assert(A_X + 4096 <= A_CH && A_X % 16 == 0, "channel image");
typedef unsigned u32x4a4 __attribute__((ext_vector_type(4), aligned(4)));
typedef float f32x4v __attribute__((ext_vector_type(4)));

constexpr unsigned A_STG = 9 * A_CH;
constexpr unsigned A_CSP = A_STG;
static_assert(A_STG + 288 * 144 <= 163840, "aux LDS");

DI void aux_role(const Params& p, LAS char* lds, const int a, u32x2 epv) {
  u64 base;
  const int tid = threadIdx.x, lane = tid & 63, wave = rfl(tid >> 6);
  const rsrc_t rws = mkrsrc(p.ws, WS_END);
  const unsigned ldsb = (unsigned)(size_t)lds;
  STAMP(0);
  constexpr unsigned OOB = 0x7fffff00u;
  const int pt = a >> 1, hf = a & 1;
  const rsrc_t rk = mkrsrc(p.k, S_ * D_ * 2), rv = mkrsrc(p.v, S_ * D_ * 2), rs = mkrsrc(p.shk, D_ * 8), rh = mkrsrc(p.ssk, D_ * S_ * 2), rw1 = mkrsrc(p.w1, D_ * F_ * 2);
  unsigned kk[3][11], vv[3][8]; u32x4 sh[3];
#pragma unroll
  for (int n = 0; n < 3; ++n) {
    const int it = tid + 512 * n, o = it / 144, cp = it - 144 * o, s0 = 64 * pt + 8 * o, ch = 288 * hf + 2 * cp;
    const bool on = it < 1152;
#pragma unroll
    for (int r = 0; r < 11; ++r) { const int sr = s0 - 3 + r; kk[n][r] = __builtin_amdgcn_raw_buffer_load_b32(rk, (on && sr >= 0) ? (unsigned)(sr * 1152 + ch * 2) : OOB, 0, 0); }
#pragma unroll
    for (int r = 0; r < 8; ++r) vv[n][r] = __builtin_amdgcn_raw_buffer_load_b32(rv, on ? (unsigned)((s0 + r) * 1152 + ch * 2) : OOB, 0, 0);
    sh[n] = __builtin_amdgcn_raw_buffer_load_b128(rs, on ? (unsigned)(ch * 8) : OOB, 0, 0);
  }
  if (tid < 9 * 36) { const int c = tid / 36, i = tid - 36 * c;
    const unsigned off = c * A_CH + (i < 2 ? i * 16 : i < 4 ? A_H1 - 32 + (i - 2) * 16 : A_X - 512 + (i - 4) * 16);
    *(LAS u32x4*)(lds + off) = u32x4{0u, 0u, 0u, 0u}; }
#pragma unroll
  for (int n = 0; n < 3; ++n) {
    const int it = tid + 512 * n, o = it / 144, cp = it - 144 * o;
    float o0[8], o1[8];
#pragma unroll
    for (int r = 0; r < 8; ++r) {
      float a0 = blo(kk[n][r]) * bhi(sh[n][1]); a0 = __builtin_fmaf(blo(kk[n][r + 1]), blo(sh[n][1]), a0); a0 = __builtin_fmaf(blo(kk[n][r + 2]), bhi(sh[n][0]), a0); a0 = __builtin_fmaf(blo(kk[n][r + 3]), blo(sh[n][0]), a0);
      float a1 = bhi(kk[n][r]) * bhi(sh[n][3]); a1 = __builtin_fmaf(bhi(kk[n][r + 1]), blo(sh[n][3]), a1); a1 = __builtin_fmaf(bhi(kk[n][r + 2]), bhi(sh[n][2]), a1); a1 = __builtin_fmaf(bhi(kk[n][r + 3]), blo(sh[n][2]), a1);
      o0[r] = rb(a0) * blo(vv[n][r]); o1[r] = rb(a1) * bhi(vv[n][r]);
    }
    if (it < 1152) {
      LAS char* dst = lds + A_STG + (2 * cp) * 144 + o * 16;
      *(LAS u32x4*)dst = u32x4{pk2(o0[7], o0[6]), pk2(o0[5], o0[4]), pk2(o0[3], o0[2]), pk2(o0[1], o0[0])};
      *(LAS u32x4*)(dst + 144) = u32x4{pk2(o1[7], o1[6]), pk2(o1[5], o1[4]), pk2(o1[3], o1[2]), pk2(o1[1], o1[0])};
    }
  }
  STAMP(8);
  base = take_epoch(p.ws, epv);
  u32x4 hA[5], hB[5];
#pragma unroll
  for (int n = 0; n < 5; ++n) {
    const int un = wave + 8 * n, c = un >> 2, i = un & 3;
    const unsigned go = un < 36 ? (unsigned)((9 * a + c) * 4096 + i * 1024 + lane * 16) : OOB;
    hA[n] = __builtin_amdgcn_raw_buffer_load_b128(rh, go, 0, 0); hB[n] = __builtin_amdgcn_raw_buffer_load_b128(rh, un < 36 ? go + 4 : OOB, 0, 0);
  }
  u32x4 wv[11];
  const int r7 = tid / 72, c72 = tid - 72 * r7;
#pragma unroll
  for (int k = 0; k < 11; ++k) {
    const int rl = r7 + 7 * k;
    wv[k] = __builtin_amdgcn_raw_buffer_load_b128(rw1, (r7 < 7 && rl < 72) ? (unsigned)((72 * (a & 7) + rl) * 9216 + (576 * (a >> 3)) * 2 + c72 * 16) : OOB, 0, 0);
  }
  sbar();
#pragma unroll
  for (int n = 0; n < 5; ++n) {
    const int gch = wave + 8 * n;
    if (gch < 36) {
      const int ch = 8 * gch + (lane >> 3), o = lane & 7;
      const u32x4 v = *(LAS u32x4*)(lds + A_STG + ch * 144 + o * 16);
      __builtin_amdgcn_raw_buffer_store_b128(v, rws, WS_KVT + (288 * hf + ch) * 4096 + (64 * pt + 8 * o) * 2, 0, 16);
    }
  }
  STAMP(1);
#pragma unroll
  for (int n = 0; n < 5; ++n) {
    const int un = wave + 8 * n, c = un >> 2, i = un & 3;
    if (un < 36) {
      const u32x4 A = hA[n], B = hB[n];
      *(LAS u32x4*)(lds + c * A_CH + A_H0 + i * 1024 + lane * 16) = A;
      *(LAS u32x4*)(lds + c * A_CH + A_H1 + i * 1024 + lane * 16) =
          u32x4{__builtin_amdgcn_alignbit(A[1], A[0], 16), __builtin_amdgcn_alignbit(A[2], A[1], 16), __builtin_amdgcn_alignbit(A[3], A[2], 16), __builtin_amdgcn_alignbit(B[3], A[3], 16)};
      if (i == 0 && lane == 0) *(LAS unsigned*)(lds + c * A_CH + A_H1 - 4) = A[0] << 16;
    }
  }
  sbar();
  STAMP(5);
  {
    float cs[8] = {0.f, 0.f, 0.f, 0.f, 0.f, 0.f, 0.f, 0.f};
#pragma unroll
    for (int k = 0; k < 11; ++k)
#pragma unroll
      for (int e = 0; e < 4; ++e) { cs[2 * e] += blo(wv[k][e]); cs[2 * e + 1] += bhi(wv[k][e]); }
    if (r7 < 7) { LAS char* dst = lds + A_CSP + (r7 * 576 + c72 * 8) * 4;
      *(LAS f32x4*)dst = f32x4{cs[0], cs[1], cs[2], cs[3]}; *(LAS f32x4*)(dst + 16) = f32x4{cs[4], cs[5], cs[6], cs[7]}; }
    STAMP(6);
    sbar();
    if (tid < 144) {
      f32x4 t = *(LAS f32x4*)(lds + A_CSP + tid * 16);
#pragma unroll
      for (int r = 1; r < 7; ++r) { const f32x4 u = *(LAS f32x4*)(lds + A_CSP + r * 2304 + tid * 16); t = f32x4{t[0] + u[0], t[1] + u[1], t[2] + u[2], t[3] + u[3]}; }
      __builtin_amdgcn_raw_buffer_store_b128(__builtin_bit_cast(u32x4, t), rws, WS_CS + ((a & 7) * F_ + 576 * (a >> 3)) * 4 + tid * 16, 0, 16);
    }
    asm volatile("s_waitcnt vmcnt(0) lgkmcnt(0)\n\ts_barrier" ::: "memory");
    if (tid == 0) flag_store(p.ws, WS_FB + a * FL_STRIDE, base + 1);
    if (tid == 64) flag_store(p.ws, WS_FA + a * FL_STRIDE, base + 1);
  }
  STAMP(2);
  {
    const int hfc = (9 * a) >= 288 ? 1 : 0;
    if (wave == 0) {
      spin_flags(p.ws, rws, WS_FB + (2 * (lane & 31) + hfc) * FL_STRIDE, lane < 32, base, 1);
      __builtin_amdgcn_fence(__ATOMIC_ACQUIRE, "agent");
      asm volatile("s_waitcnt vmcnt(0)" ::: "memory");
    }
    sbar();
#pragma unroll
    for (int n = 0; n < 5; ++n) {
      const int un = wave + 8 * n, c = un >> 2, i = un & 3;
      if (un < 36) dma16(rfl(ldsb + c * A_CH + A_X + i * 1024), lane * 16, rws, rfl(WS_KVT + (9 * a + c) * 4096 + i * 1024));
    }
    asm volatile("s_waitcnt vmcnt(0) lgkmcnt(0)\n\ts_barrier" ::: "memory");
  }
  STAMP(3);
  const int b = lane & 15, g = lane >> 4;
  const int odd = (b + 1) & 1;
  const unsigned aH = (odd ? A_H1 : A_H0) + 2 * (9 + b - 8 * g - odd);
  const unsigned aX = A_X + 32 * b - 32 + 16 * g;
  auto rdH = [&](unsigned chb, int mp) -> bf16x8 { return __builtin_bit_cast(bf16x8, (u32x4)*(LAS u32x4a4*)(lds + chb + aH + 64 * mp)); };
  auto rdX = [&](unsigned chb, int n, int kappa) -> bf16x8 { return *(LAS bf16x8*)(lds + chb + aX + 512 * n - 64 * kappa); };
  const float dd0 = b2f(p.ssd[9 * a + wave]), dd8 = b2f(p.ssd[9 * a + 8]);
  auto finish = [&](int c, int t, f32x4v y) {
    const u32x2 kvr = *(LAS u32x2*)(lds + c * A_CH + A_X + (32 * t + 2 * b + (g >> 1)) * 16 + ((g & 1) ? 0 : 8));
    const float dd = c == 8 ? dd8 : dd0;
    const float z0 = rb(y[0]) + rb(bhi(kvr[1]) * dd), z1 = rb(y[1]) + rb(blo(kvr[1]) * dd), z2 = rb(y[2]) + rb(bhi(kvr[0]) * dd), z3 = rb(y[3]) + rb(blo(kvr[0]) * dd);
    __builtin_amdgcn_raw_buffer_store_b64(u32x2{pk2(z0, z1), pk2(z2, z3)}, rws, WS_ZT + (9 * a + c) * 4096 + (256 * t + 16 * b + 4 * g) * 2, 0, 16);
  };
  {
    const unsigned chb = wave * A_CH;
    f32x4v acc[8];
#pragma unroll
    for (int t = 0; t < 8; ++t) acc[t] = f32x4v{0.f, 0.f, 0.f, 0.f};
    bf16x8 H[2][8], X[2][8];
#pragma unroll
    for (int m = 0; m < 8; ++m) { H[0][m] = rdH(chb, 8 * m); X[0][m] = rdX(chb, m, 0); }
#pragma unroll 1
    for (int kp = 0; kp < 8; kp += 2) {
#pragma unroll
      for (int hb = 0; hb < 2; ++hb) {
        const int kn = kp + hb + 1;
#pragma unroll
        for (int m = 0; m < 8; ++m) {
          H[hb ^ 1][m] = rdH(chb + 64 * (kn & 7), 8 * m); X[hb ^ 1][m] = rdX(chb - 64 * (kn & 7), m, 0);
#pragma unroll
          for (int t = m; t < 8; ++t) acc[t] = __builtin_amdgcn_mfma_f32_16x16x32_bf16(H[hb][m], X[hb][t - m], acc[t], 0, 0, 0);
        }
      }
    }
#pragma unroll
    for (int t = 0; t < 8; ++t) finish(wave, t, acc[t]);
  }
  STAMP(9);
  {
    const unsigned chb = 8 * A_CH;
    f32x4v acc0 = {0.f, 0.f, 0.f, 0.f}, acc1 = acc0;
    const int t = wave;
    bf16x8 H[2][4], X[2][4];
    auto rd4 = [&](int hb, int g4) {
#pragma unroll
      for (int k = 0; k < 4; ++k) { H[hb][k] = rdH(chb + 256 * g4, k); X[hb][k] = *(LAS bf16x8*)(lds + chb + aX + 512 * t - 256 * g4 - 64 * k); }
    };
    rd4(0, 0);
#pragma unroll 1
    for (int g8 = 0; g8 <= t; ++g8) {
      rd4(1, 2 * g8 + 1);
#pragma unroll
      for (int k = 0; k < 4; k += 2) { acc0 = __builtin_amdgcn_mfma_f32_16x16x32_bf16(H[0][k], X[0][k], acc0, 0, 0, 0); acc1 = __builtin_amdgcn_mfma_f32_16x16x32_bf16(H[0][k + 1], X[0][k + 1], acc1, 0, 0, 0); }
      rd4(0, g8 < t ? 2 * g8 + 2 : 0);
#pragma unroll
      for (int k = 0; k < 4; k += 2) { acc0 = __builtin_amdgcn_mfma_f32_16x16x32_bf16(H[1][k], X[1][k], acc0, 0, 0, 0); acc1 = __builtin_amdgcn_mfma_f32_16x16x32_bf16(H[1][k + 1], X[1][k + 1], acc1, 0, 0, 0); }
    }
    finish(8, t, acc0 + acc1);
  }
  publish_flag(p.ws, WS_FZ + a * FL_STRIDE, base + 1);
  STAMP(4);
  if (a == 63 && wave == 0) {
#pragma unroll 1
    for (int k = 0; k < 4; ++k) spin_flags(p.ws, rws, WS_ARR + (64 * k + lane) * FL_STRIDE, true, base, 0);
    if (lane == 0) flag_store(p.ws, WS_EPOCH, base >> 4);
  }
}

__global__ __launch_bounds__(512, 2) void k_main(Params p) {
  extern __shared__ __attribute__((aligned(16))) char smem[];
  LAS char* lds = (LAS char*)smem;
  const int b = blockIdx.x, g = b & 7, qq = b >> 3;
  u32x2 epv;
  asm volatile("global_load_dwordx2 %0, %1, %2 sc1" : "=v"(epv) : "v"(0u), "s"(p.ws) : "memory");
  if (qq < 24) mlp_role(p, lds, 8 * (g & 1) + qq / 3, g >> 1, qq % 3, epv);
  else aux_role(p, lds, (qq - 24) * 8 + g, epv);
}
}
#if defined(__HIP_DEVICE_COMPILE__)
#pragma clang attribute pop
#endif

extern "C" void kernel_launch(void* const* d_in, const int* in_sizes, int n_in, void* d_out, int out_size, void* d_ws, size_t ws_size, hipStream_t stream) {
  using namespace mk;
  Params p;
  p.x = (const u16*)d_in[0]; p.k = (const u16*)d_in[1]; p.v = (const u16*)d_in[2]; p.shk = (const u16*)d_in[3]; p.ssk = (const u16*)d_in[4]; p.ssd = (const u16*)d_in[5];
  p.w1 = (const u16*)d_in[7]; p.b1 = (const u16*)d_in[8]; p.w2 = (const u16*)d_in[9]; p.b2 = (const u16*)d_in[10];
  p.out = (u16*)d_out; p.ws = (char*)d_ws;
  (void)hipFuncSetAttribute((const void*)k_main, hipFuncAttributeMaxDynamicSharedMemorySize, LDS_BYTES);
  k_main<<<256, 512, LDS_BYTES, stream>>>(p);
}
```

```cpp
#include <hip/hip_runtime.h>
#include <stdint.h>
#include <type_traits>

#if defined(__HIP_DEVICE_COMPILE__)
#pragma clang attribute push(__attribute__((target("no-packed-fp32-ops"))), apply_to = function)
#endif
namespace mk {
constexpr int S_ = 2048, D_ = 576, F_ = 4608;
typedef unsigned short u16;
typedef unsigned long long u64;
typedef __bf16 bf16x8 __attribute__((ext_vector_type(8)));
typedef __bf16 bf16x2 __attribute__((ext_vector_type(2)));
typedef short s16x4 __attribute__((ext_vector_type(4)));
typedef short s16x8 __attribute__((ext_vector_type(8)));
typedef float f32x16 __attribute__((ext_vector_type(16)));
typedef float f32x4 __attribute__((ext_vector_type(4)));
typedef float f32x2 __attribute__((ext_vector_type(2)));
typedef unsigned u32x4 __attribute__((ext_vector_type(4)));
typedef unsigned u32x2 __attribute__((ext_vector_type(2)));
#define LAS __attribute__((address_space(3)))
#define GAS __attribute__((address_space(1)))
#define DI __device__ __forceinline__
typedef __amdgpu_buffer_rsrc_t rsrc_t;

constexpr unsigned FL_STRIDE = 128;
constexpr unsigned WS_EPOCH = 0;
constexpr unsigned WS_ARR   = 1024;
constexpr unsigned WS_PROG  = WS_ARR + 256 * FL_STRIDE;
constexpr unsigned WS_PFL   = WS_PROG + 192 * FL_STRIDE;
constexpr unsigned WS_FA    = WS_PFL + 768 * FL_STRIDE;
constexpr unsigned WS_FB    = WS_FA + 64 * FL_STRIDE;
constexpr unsigned WS_FZ    = WS_FB + 64 * FL_STRIDE;
constexpr unsigned WS_TMO   = WS_FZ + 64 * FL_STRIDE;
constexpr unsigned WS_FLAGS_END = 262144;
static_assert(WS_TMO + 128 <= WS_FLAGS_END, "flags");
constexpr unsigned WS_CS    = WS_FLAGS_END;
constexpr unsigned WS_KVT   = WS_CS + 8 * F_ * 4;
constexpr unsigned WS_ZT    = WS_KVT + D_ * S_ * 2;
constexpr unsigned WS_H     = WS_ZT + D_ * S_ * 2;
constexpr unsigned WS_PART  = WS_H + S_ * F_ * 2;
constexpr unsigned WS_STAMP = WS_PART + 48 * 16 * 12288;
constexpr unsigned WS_TL    = WS_STAMP + 256 * 64 * 8;
constexpr unsigned WS_END   = WS_TL + 256 * 1024 * 8;

struct Params {
  const u16 *x, *k, *v, *shk, *ssk, *ssd, *w1, *b1, *w2, *b2;
  u16* out; char* ws;
};

template <int I> using IC = std::integral_constant<int, I>;
template <int B, int E, class F> DI void sfor(F&& f) { if constexpr (B < E) { f(IC<B>{}); sfor<B + 1, E>(f); } }

DI float asf(unsigned u) { return __builtin_bit_cast(float, u); }
DI float b2f(u16 u) { return asf((unsigned)u << 16); }
DI float blo(unsigned u) { return asf(u << 16); }
DI float bhi(unsigned u) { return asf(u & 0xffff0000u); }
DI unsigned pk2(float a, float b) { f32x2 v = {a, b}; return __builtin_bit_cast(unsigned, __builtin_convertvector(v, bf16x2)); }
DI float rb(float f) { return blo(pk2(f, 0.f)); }
DI rsrc_t mkrsrc(const void* p, unsigned bytes) { return __builtin_amdgcn_make_buffer_rsrc((void*)p, 0, bytes, 0x00020000); }
DI unsigned rfl(unsigned v) { return __builtin_amdgcn_readfirstlane(v); }

DI void dma16(unsigned ldsaddr, unsigned voff, rsrc_t srd, unsigned soff) {
  asm volatile("s_mov_b32 m0, %0\n\ts_nop 0\n\tbuffer_load_dwordx4 %1, %2, %3 offen lds" :: "s"(ldsaddr), "v"(voff), "s"(srd), "s"(soff) : "memory");
}
template <unsigned MI, unsigned SI> DI void dma16i(unsigned mb, unsigned voff, rsrc_t srd, unsigned sb) {
  unsigned tmp;
  asm volatile("s_add_u32 m0, %1, %5\n\ts_add_u32 %0, %4, %6\n\tbuffer_load_dwordx4 %2, %3, %0 offen lds"
               : "=&s"(tmp) : "s"(mb), "v"(voff), "s"(srd), "s"(sb), "i"(MI), "i"(SI) : "scc", "memory");
}
template <unsigned SI> DI void ld16i_sc1(u32x4& dst, unsigned voff, rsrc_t srd, unsigned sb) {
  unsigned tmp;
  asm volatile("s_add_u32 %1, %4, %5\n\tbuffer_load_dwordx4 %0, %2, %3, %1 offen sc1" : "=v"(dst), "=&s"(tmp) : "v"(voff), "s"(srd), "s"(sb), "i"(SI) : "scc", "memory");
}
template <int WHICH> DI void dma16_if(unsigned sel, unsigned ldsaddr, unsigned voff, rsrc_t srd, unsigned soff) {
  asm volatile("s_cmp_lg_u32 %4, %5\n\ts_cbranch_scc1 .Ldma%=\n\ts_mov_b32 m0, %0\n\ts_nop 0\n\tbuffer_load_dwordx4 %1, %2, %3 offen lds\n.Ldma%=:"
               :: "s"(ldsaddr), "v"(voff), "s"(srd), "s"(soff), "s"(sel), "i"(WHICH) : "scc", "memory");
}
DI void ld16_sc1(u32x4& dst, unsigned voff, rsrc_t srd, unsigned soff) {
  asm volatile("buffer_load_dwordx4 %0, %1, %2, %3 offen sc1" : "=v"(dst) : "v"(voff), "s"(srd), "s"(soff) : "memory");
}
DI void ld16_sc1_s(u32x4& dst, unsigned voff, rsrc_t srd, unsigned soff) {
  asm volatile("s_nop 4\n\tbuffer_load_dwordx4 %0, %1, %2, %3 offen sc1" : "=v"(dst) : "v"(voff), "s"(srd), "s"(rfl(soff)) : "memory");
}
DI void ld8_sc1_s(u32x2& dst, unsigned voff, rsrc_t srd, unsigned soff) {
  asm volatile("s_nop 4\n\tbuffer_load_dwordx2 %0, %1, %2, %3 offen sc1" : "=v"(dst) : "v"(voff), "s"(srd), "s"(rfl(soff)) : "memory");
}
DI void ld8_sc1(u32x2& dst, unsigned voff, rsrc_t srd, unsigned soff) {
  asm volatile("buffer_load_dwordx2 %0, %1, %2, %3 offen sc1" : "=v"(dst) : "v"(voff), "s"(srd), "s"(soff) : "memory");
}
DI void ld4_sc1(unsigned& dst, unsigned voff, rsrc_t srd, unsigned soff) {
  asm volatile("buffer_load_dword %0, %1, %2, %3 offen sc1" : "=v"(dst) : "v"(voff), "s"(srd), "s"(soff) : "memory");
}
template <int N> DI void waitv() { asm volatile("s_waitcnt vmcnt(%0)" :: "i"(N) : "memory"); }
DI void bar() { __builtin_amdgcn_sched_barrier(0); asm volatile("s_barrier" ::: "memory"); __builtin_amdgcn_sched_barrier(0); }
DI void lgkm0() { asm volatile("s_waitcnt lgkmcnt(0)" ::: "memory"); }

DI int flane() { int l; asm volatile("v_mbcnt_lo_u32_b32 %0, -1, 0\n\tv_mbcnt_hi_u32_b32 %0, -1, %0" : "=v"(l)); return l; }
DI bool wall(bool c) { return __builtin_amdgcn_ballot_w64(!c) == 0; }
DI bool flag_ok(u32x2 v, u64 base, unsigned need) { u64 d = (((u64)v.y << 32) | v.x) - base; return d >= need && d <= 15; }
DI void flag_store(char* ws, unsigned off, u64 val) { __hip_atomic_store((GAS u64*)(ws + off), val, __ATOMIC_RELAXED, __HIP_MEMORY_SCOPE_AGENT); }
DI void spin_flags(char* ws, rsrc_t wsr, unsigned voff, bool active, u64 base, unsigned need) {
  const unsigned tag = (unsigned)base | 1u;
  for (unsigned it = 0;; ++it) {
    u32x2 v; ld8_sc1(v, voff, wsr, 0); asm volatile("s_waitcnt vmcnt(0)" : "+v"(v) :: "memory");
    if (wall(!active || flag_ok(v, base, need))) return;
    if (it > 30000u) { __hip_atomic_store((GAS unsigned*)(ws + WS_TMO), tag, __ATOMIC_RELAXED, __HIP_MEMORY_SCOPE_AGENT); return; }
    if ((it & 255u) == 255u && rfl(__hip_atomic_load((GAS unsigned*)(ws + WS_TMO), __ATOMIC_RELAXED, __HIP_MEMORY_SCOPE_AGENT)) == tag) return;
    __builtin_amdgcn_s_sleep(1);
  }
}
DI void sbar() { asm volatile("s_waitcnt lgkmcnt(0)\n\ts_barrier" ::: "memory"); }
DI void publish_flag(char* ws, unsigned off, u64 val) {
  asm volatile("s_waitcnt vmcnt(0) lgkmcnt(0)\n\ts_barrier" ::: "memory");
  if (threadIdx.x == 0) flag_store(ws, off, val);
}
DI u64 take_epoch(char* ws, const u32x2& epv) {
  unsigned lo, hi;
  asm volatile("v_readfirstlane_b32 %0, %2\n\tv_readfirstlane_b32 %1, %3\n\ts_nop 3" : "=s"(lo), "=s"(hi) : "v"(epv.x), "v"(epv.y) : "memory");
  const u64 base = ((((u64)hi << 32) | lo) + 1) << 4;
  if (threadIdx.x == 0) flag_store(ws, WS_ARR + blockIdx.x * FL_STRIDE, base);
  return base;
}
DI float wave_xor_add8(float v) {
  v += __builtin_bit_cast(float, __builtin_amdgcn_update_dpp(0, __builtin_bit_cast(int, v), 0xB1, 0xf, 0xf, true));
  v += __builtin_bit_cast(float, __builtin_amdgcn_update_dpp(0, __builtin_bit_cast(int, v), 0x4E, 0xf, 0xf, true));
  v += __builtin_bit_cast(float, __builtin_amdgcn_update_dpp(0, __builtin_bit_cast(int, v), 0x141, 0xf, 0xf, true));
  return v;
}

constexpr unsigned R1 = 0;
constexpr unsigned R2 = 9 * 8192;
constexpr unsigned L_MISC = R2 + 73728;
constexpr unsigned L_AUG = L_MISC;
constexpr unsigned L_B1RAW = L_AUG + 6144;
constexpr unsigned L_B2RAW = L_B1RAW + 1024;
constexpr unsigned L_HO = L_B2RAW + 1024;
constexpr unsigned L_ST = R2 + 65536;
constexpr unsigned L_Z = R2;
constexpr unsigned L_XF = L_Z + 12288;
constexpr unsigned LDS_BYTES = 163840;

#define STAMP(n) do {} while (0)

#define TLK(T, K) do {} while (0)
#define TL(T) do {} while (0)
#define TLDUMP() do {} while (0)
namespace sch {
constexpr int NB = 18;
constexpr int TP = 36;
constexpr int kSel[NB]    = {    0,  0,  0,  1,  2,  0,  1,  2,  1,  2,  0,  1,  2,  0,  1,  2,  1,  2 };
constexpr int kUnit[NB]   = {    1,  0,  2,  1,  1,  3,  0,  0,  2,  2,  4,  3,  3,  5,  4,  4,  5,  5 };
constexpr int kStart[NB]  = {   12, 17, 20, 23, 26, 29, 32, 35, 38, 41, 44, 47, 50, 53, 56, 59, 62, 65 };
constexpr int kRing[NB]   = {   -1, -1, -1,  0,  1, -1,  2,  0,  1,  2, -1,  0,  1, -1,  2,  0,  1,  2 };
constexpr int kHIssue[NB] = {   -1, -1, -1, 20, 21, -1, 27, 29, 32, 35, -1, 41, 44, -1, 50, 53, 56, 59 };
constexpr int kW2Buf[NB]  = {    3,  4,  5,  3,  4,  5,  3,  4,  5,  0,  1,  2,  3,  4,  5,  0,  1,  2 };
constexpr int bsel(int b) { return kSel[b]; }
constexpr int bunit(int b) { return kUnit[b]; }
constexpr int bstart(int b) { return kStart[b]; }
constexpr int NST = bstart(NB - 1) + 3;
constexpr int TZ1 = 59, TZ2 = 65;
constexpr int g_ks(int b, int G) { return b == NB - 1 ? G % 4 : G / 6; }
constexpr int g_tn(int b, int G) { return b == NB - 1 ? G / 4 : G % 6; }
constexpr int blk_of(int T) { for (int b = 0; b < NB; ++b) if (T >= bstart(b) && T < bstart(b) + 3) return b; return -1; }
constexpr bool by_P(int u) { return u != 1 && u != 2; }
constexpr int gelu0(int u) { return u == 0 ? 9 : u == 5 ? TP : 9 * u - 9; }
constexpr int chunkW(int u, int k) { return u == 5 ? TP + 1 + k : gelu0(u) + 2 + 2 * k; }
constexpr int chunkR(int u, int k) { return chunkW(u, k) + 1; }
constexpr bool ho_written(int T) { for (int u = 0; u < 6; ++u) if (by_P(u)) for (int k = 0; k < 4; ++k) if (chunkW(u, k) == T) return true; return false; }
constexpr int cr_unit(int T) { for (int u = 0; u < 6; ++u) if (by_P(u)) for (int k = 0; k < 4; ++k) if (chunkR(u, k) == T) return u; return -1; }
constexpr int cr_k(int T) { for (int u = 0; u < 6; ++u) if (by_P(u)) for (int k = 0; k < 4; ++k) if (chunkR(u, k) == T) return k; return -1; }
constexpr int ownset(int u) { return (u == 1 || u == 2 || u == 4) ? 0 : 1; }
constexpr int pubK(int T) { return T == 16 ? 1 : T == 23 ? 2 : T == 25 ? 3 : T == 32 ? 4 : T == 41 ? 5 : T == 46 ? 6 : 0; }
constexpr int needOf(int u) { return u == 1 ? 1 : u == 0 ? 2 : u + 1; }
constexpr int TG2 = 12;
constexpr int c1_unit(int c) { return c < 27 ? c % 3 : 3 + (c - 27) / 9; }
constexpr int c1_kc(int c) { return c < 27 ? c / 3 : (c - 27) % 9; }
constexpr int c1_use(int c) { return c < 27 ? c / 3 : c - 18; }
#define W1LEAD 6
constexpr int c1_issue(int c) { return c < 9 ? -1 : c < 33 ? c / 3 - 2 : c < 37 ? 11 + (c - 33) / 2 : c - 18 - W1LEAD; }
constexpr int x_issue(int kc) { return kc < 4 ? -1 : kc - 3; }
constexpr int x_pos(int kc) { return (kc + 3) & 3; }
constexpr int w2_issue(int b) { return b == 0 ? 7 : b == 1 ? 11 : b <= 8 ? bstart(b) - 6 : bstart(b) - 9; }
constexpr int w2_buf(int b) { return kW2Buf[b]; }
constexpr int h_issue(int b) { return kHIssue[b]; }
constexpr bool dma_by_C(int T) { return T < 12; }
constexpr int nD(int T, bool C) {
  int n = 0;
  for (int c = 9; c < 54; ++c) if (c1_issue(c) == T) n += T < 9 ? 1 : (dma_by_C(T) == C) ? 2 : 0;
  for (int kc = 4; kc < 9; ++kc) if (x_issue(kc) == T) n += 2;
  for (int b = 0; b < NB; ++b) if (T >= w2_issue(b) && T < w2_issue(b) + 3) n += b == 0 ? 1 : (dma_by_C(T) == C) ? 2 : 0;
  return n;
}
constexpr int nH(int T) { int n = 0; for (int b = 0; b < NB; ++b) if (h_issue(b) >= 0 && (h_issue(b) == T || h_issue(b) + 1 == T)) n += 2; return n; }
constexpr int nS(int T) { return ((T >= 12 && cr_unit(T) >= 0) ? 1 : 0) + ((T == TG2 + 2 || T == TG2 + 4 || T == TG2 + 6 || T == TG2 + 8) ? 1 : 0); }
constexpr int cnt(int T, bool C) { return nD(T, C) + (C ? nH(T) + nS(T) : 0); }
constexpr int after(int t0, int T, bool C) { int n = 0; for (int t = t0 + 1; t < T; ++t) n += cnt(t, C); return n; }
constexpr int req(int T, bool C) {
  int r = -2;
  auto mx = [&r](int v) { r = r > v ? r : v; };
  for (int tt = T; tt <= T + 1; ++tt) {
    if (tt == T + 1 && T < 9) break;
    if (tt < 9) { mx(c1_issue(3 * tt)); mx(c1_issue(3 * tt + 1)); mx(c1_issue(3 * tt + 2)); mx(x_issue(tt)); }
    else {
      if (tt < TP) { const int t = c1_issue(tt + 18); if (t < 9 || dma_by_C(t) == C) mx(t); }
      const int b = blk_of(tt);
      if (b >= 0) for (int k = 0; k < 3; ++k) { const int t = w2_issue(b) + k; if (b == 0 || dma_by_C(t) == C) mx(t); }
    }
  }
  if (C) { const int b = blk_of(T); if (b >= 0 && bstart(b) == T && h_issue(b) >= 0) mx(h_issue(b) + 1); }
  return r;
}
constexpr int c1_at(int T, int n) { for (int c = 9; c < 54; ++c) if (c1_issue(c) == T) { if (n == 0) return c; --n; } return -1; }
constexpr int x_at(int T) { for (int kc = 4; kc < 9; ++kc) if (x_issue(kc) == T) return kc; return -1; }
constexpr int w2_at(int T, int k = 0) { for (int b = 0; b < NB; ++b) if (T >= w2_issue(b) && T < w2_issue(b) + 3) { if (k == 0) return b; --k; } return -1; }
constexpr int h_at(int T) { for (int b = 0; b < NB; ++b) if (h_issue(b) >= 0 && h_issue(b) == T) return b; return -1; }
constexpr int chk_at(int T) { const int b = h_at(T); return (b >= 0 && bsel(b) == 1) ? b : -1; }
constexpr int val_at(int G, int G0, int RN, int RD, int k) { for (int v = 0; v < 32; ++v) if (G0 + v * RN / RD == G) { if (k == 0) return v; --k; } return -1; }
constexpr int PRO = 9 + 8;
constexpr int waitn(int T, bool C) {
  const int r = req(T, C);
  if (r == -2) return 63;
  if (r >= 0) return after(r, T, C);
  const int need = T < 3 ? 5 * (T + 1) : PRO;
  return PRO - need + after(-1, T, C);
}
constexpr bool check() {
  if (cnt(0, true) != 0 || cnt(0, false) != 0) return false;
  for (int c = 9; c < 54; ++c) {
    if (c1_issue(c) <= c1_use(c - 9)) return false;
    if (c1_issue(c) > c1_use(c) - (c < 27 ? 1 : 2)) return false;
  }
  for (int T = 9; T < NST; ++T) { if (c1_at(T, 2) >= 0 || w2_at(T, 2) >= 0) return false; }
  for (int T = 0; T < 9; ++T) if (c1_at(T, 3) >= 0) return false;
  for (int b = 0; b < NB; ++b) {
    for (int o = 0; o < b; ++o) if (w2_buf(o) == w2_buf(b) && bstart(o) + 3 > w2_issue(b)) return false;
    if (w2_issue(b) + 2 > bstart(b) - 2) return false;
    if (w2_buf(b) < 3) for (int c = 45; c < 54; ++c) if ((c % 9) / 3 == w2_buf(b) && c1_use(c) >= w2_issue(b)) return false;
    if (h_issue(b) >= 0) {
      if (h_issue(b) + 1 > bstart(b) - 2) return false;
      for (int o = 0; o < b; ++o) if (kRing[o] == kRing[b] && bstart(o) + 3 > h_issue(b)) return false;
    }
  }
  for (int T = 0; T < NST; ++T) if (waitn(T, true) > 63 || waitn(T, false) > 63) return false;
  return true;
}
static_assert(check(), "schedule tables");
}

DI void mlp_role(const Params& p, LAS char* lds, const int i, const int s, const int jn, u32x2 epv) {
  u64 base = 0;
  using namespace sch;
  const int tid = threadIdx.x, lane = tid & 63;
  const int wave = rfl(tid >> 6), rg = wave & 3, role = wave >> 2;
  const int j = 3 * s + jn;
  const int m = lane & 31, hh = lane >> 5, blk = (lane >> 4) & 1, q = (lane & 15) >> 2, pp = lane & 3;
  const rsrc_t rx = mkrsrc(p.x, S_ * D_ * 2), rw1 = mkrsrc(p.w1, D_ * F_ * 2), rw2 = mkrsrc(p.w2, F_ * D_ * 2);
  const rsrc_t rws = mkrsrc(p.ws, WS_END);
  const unsigned ldsb = (unsigned)(size_t)lds;
  STAMP(0);
  const unsigned voX = (8 * wave + (lane >> 3)) * 1152 + (((lane & 7) ^ ((4 * (wave & 1) + (lane >> 4)) & 7)) * 16);
  const unsigned voW1 = (8 * wave + (lane >> 3)) * 9216 + (((lane & 7) ^ (((lane >> 4) & 1) << 2)) * 16);
  unsigned voW2[3];
#pragma unroll
  for (int ii = 0; ii < 3; ++ii) { const unsigned L = 1024 * (3 * wave + ii) + 16 * lane, row = L / 384, ch = (L % 384) / 16; voW2[ii] = row * 1152 + ((ch ^ ((row >> 1) & 1)) * 16); }
  const int jA = 3 * s + (jn + 1) % 3, jB = 3 * s + (jn + 2) % 3;
  const unsigned mW = ldsb + wave * 1024, mW3 = ldsb + wave * 3072, mC2 = ldsb + rg * 2048, mC6 = ldsb + rg * 6144;
  const unsigned sW1 = 768 * j, sX = 128 * i * 1152;
  const unsigned sW2[3] = {(unsigned)(384 * j * 1152 + 384 * jn), (unsigned)(384 * jA * 1152 + 384 * jn), (unsigned)(384 * jB * 1152 + 384 * jn)};
  auto dmaW1 = [&](auto Cc) { constexpr int c = decltype(Cc)::value;
    dma16i<R1 + (c % 9) * 8192, (64 * c1_kc(c)) * 9216 + 128 * c1_unit(c)>(mW, voW1, rw1, sW1); };
  auto dmaX = [&](auto Kc) { constexpr int kc = decltype(Kc)::value;
    dma16i<R2 + x_pos(kc) * 16384, kc * 128>(mW, voX, rx, sX);
    dma16i<R2 + x_pos(kc) * 16384 + 8192, 64 * 1152 + kc * 128>(mW, voX, rx, sX); };
  auto dmaW2 = [&](auto Bc, auto Kc) { constexpr int b = decltype(Bc)::value, k = decltype(Kc)::value;
    dma16i<w2_buf(b) * 24576 + k * 1024, 64 * bunit(b) * 1152>(mW3, voW2[k], rw2, sW2[bsel(b)]); };
  const unsigned voW1c = (16 * rg + (lane >> 3)) * 9216 + (((lane & 7) ^ (((lane >> 4) & 1) << 2)) * 16);
  unsigned voW2c[3];
  auto mkW2c = [&](int l) {
#pragma unroll
    for (int ii = 0; ii < 3; ++ii) { const unsigned L = 1024 * ii + 16 * l, row = L / 384, ch = (L % 384) / 16; voW2c[ii] = (16 * rg + row) * 1152 + ((ch ^ ((row >> 1) & 1)) * 16); }
  };
  auto dmaW1c = [&](auto Cc, auto Hc) { constexpr int c = decltype(Cc)::value, h = decltype(Hc)::value;
    dma16i<R1 + (c % 9) * 8192 + h * 1024, (64 * c1_kc(c) + 8 * h) * 9216 + 128 * c1_unit(c)>(mC2, voW1c, rw1, sW1); };
  auto dmaW2c = [&](auto Bc, auto Kc) { constexpr int b = decltype(Bc)::value, k = decltype(Kc)::value;
    dma16i<w2_buf(b) * 24576 + k * 1024, (64 * bunit(b) + 8 * (k / 3)) * 1152>(mC6, voW2c[k % 3], rw2, sW2[bsel(b)]); };
  auto site = [&](auto Tc, auto Sc, auto Cr) {
    constexpr int T = decltype(Tc)::value, st = decltype(Sc)::value; constexpr bool C = decltype(Cr)::value;
    if constexpr (T < 9) {
      if constexpr (st < 3 && c1_at(T, st) >= 0) dmaW1(IC<c1_at(T, st)>{});
      if constexpr (st == 3 && x_at(T) >= 0) dmaX(IC<x_at(T)>{});
    } else if constexpr (dma_by_C(T) == C) {
      if constexpr (st < 2 && c1_at(T, 0) >= 0) dmaW1c(IC<c1_at(T, 0)>{}, Sc);
      if constexpr (st < 2 && c1_at(T, 1) >= 0) dmaW1c(IC<c1_at(T, 1)>{}, Sc);
    }
    if constexpr (w2_at(T) == 0) { if constexpr (st == 3) dmaW2(IC<0>{}, IC<T - w2_issue(0)>{}); }
    else if constexpr (dma_by_C(T) == C && w2_at(T) > 0 && st >= 2) dmaW2c(IC<w2_at(T)>{}, IC<2 * (T - w2_issue(w2_at(T) < 0 ? 0 : w2_at(T))) + (st - 2)>{});
    if constexpr (dma_by_C(T) == C && w2_at(T, 1) > 0 && st < 2) dmaW2c(IC<w2_at(T, 1)>{}, IC<2 * (T - w2_issue(w2_at(T, 1) < 0 ? 0 : w2_at(T, 1))) + st>{});
  };
  auto allsites = [&](auto Tc, auto Cr) { site(Tc, IC<0>{}, Cr); site(Tc, IC<1>{}, Cr); site(Tc, IC<2>{}, Cr); site(Tc, IC<3>{}, Cr); };
  if (wave == 0) dma16(ldsb + L_B1RAW, lane * 16, mkrsrc(p.b1 + 384 * j, 768), 0);
  if (wave == 1) dma16(ldsb + L_B2RAW, lane * 16, mkrsrc(p.b2 + 192 * jn, 384), 0);
  sfor<0, 3>([&](auto Kc) { constexpr int k = decltype(Kc)::value; dmaX(Kc); dmaW1(IC<3 * k>{}); dmaW1(IC<3 * k + 1>{}); dmaW1(IC<3 * k + 2>{}); });
  dmaX(IC<3>{});

  const int sw = (q >> 1) & 1;
  const unsigned a1row = ldsb + R1 + (8 * hh + q) * 128 + 32 * blk + 8 * pp;
  const unsigned a1e = a1row + 64 * sw, a1o = a1row + 64 * (1 - sw);
  const unsigned a2row = ldsb + (4 * hh + q) * 384 + 96 * (2 * blk + (pp >> 1)) + 8 * (pp & 1);
  const unsigned a2e = a2row + 16 * sw, a2o = a2row + 16 * (1 - sw);
  const unsigned ax = ldsb + R2 + (32 * rg + m) * 128 + ((hh ^ ((m >> 1) & 7)) * 16);
  auto trr = [&](unsigned addr, unsigned d) -> bf16x8 {
    s16x4 lo = __builtin_amdgcn_ds_read_tr16_b64_v4i16((LAS s16x4*)(size_t)addr);
    s16x4 hi = __builtin_amdgcn_ds_read_tr16_b64_v4i16((LAS s16x4*)(size_t)(addr + d));
    return __builtin_bit_cast(bf16x8, __builtin_shufflevector(lo, hi, 0, 1, 2, 3, 4, 5, 6, 7));
  };
  const f32x16 zero16 = {};
  const unsigned hbase_own = WS_H + (unsigned)(i * 12 + j) * 98304u;
  const unsigned voH = rg * 4096 + lane * 16;
  float sd = 1.f, k0 = 0.f, k1 = 0.f, mean_f = 0.f;
  bf16x8 augB = {};
  u32x2 pollv = {0u, 0u};
  float st2[4] = {0.f, 0.f, 0.f, 0.f};
  f32x4 st1 = {0.f, 0.f, 0.f, 0.f};
  unsigned csv[8];
  auto actFA = [&](int l) { return l < 16 && (384 * j) / 576 + (l >> 3) <= (384 * j + 383) / 576; };
  auto voFA = [&](int l) -> unsigned { return actFA(l) ? WS_FA + (((384 * j) / 576 + (l >> 3)) * 8 + (l & 7)) * FL_STRIDE : WS_FA; };

  bf16x8 xr[36];
  f32x16 acc[2][2];
#define PRIOWIN 12
#define PFP_ 4
  constexpr int PF = 4, PFP = PFP_;
  bf16x8 pf[PF];
  unsigned b1e = a1e + role * 8192, b1o = a1o + role * 8192, b1en = 0, b1on = 0;
  asm volatile("" : "+v"(b1e), "+v"(b1o));
  auto rd1 = [&](auto Ic, auto NUc) -> bf16x8 { constexpr int I = decltype(Ic)::value, NU = decltype(NUc)::value;
    return trr(((I & 1) ? b1o : b1e) + ((I >> 1) % NU) * 8192 + (I / (2 * NU)) * 2048, 512); };
  auto rd1n = [&](auto Dc, auto Ic) -> bf16x8 { constexpr int D = decltype(Dc)::value, I = decltype(Ic)::value;
    if constexpr (D > 0) return trr(((I & 1) ? b1o : b1e) + D * 8192 + (I >> 1) * 2048, 512);
    else return trr(((I & 1) ? b1on : b1en) + (I >> 1) * 2048, 512); };
  auto adv1 = [&](auto Dc) { constexpr int D = decltype(Dc)::value; b1e += D * 8192; b1o += D * 8192; asm volatile("" : "+v"(b1e), "+v"(b1o)); };
  u32x4 hv[4];
  const unsigned aHO = ldsb + L_HO + rg * 1024 + lane * 16;
  float ez[32], eo[32];
  auto epigap = [&](auto Uc, auto Bc, auto Gc, auto G0c, auto RNc, auto RDc) {
    constexpr int uu = decltype(Uc)::value, BUF = decltype(Bc)::value, G = decltype(Gc)::value, G0 = decltype(G0c)::value, RN = decltype(RNc)::value, RD = decltype(RDc)::value;
    sfor<0, 2>([&](auto Kc) { constexpr int v = val_at(G - 3, G0, RN, RD, decltype(Kc)::value); if constexpr (v >= 0) ez[v] = __builtin_fmaf(ez[v], sd, sd); });
    sfor<0, 2>([&](auto Kc) { constexpr int v = val_at(G, G0, RN, RD, decltype(Kc)::value);
      if constexpr (v >= 0) { const float w = acc[BUF][v >> 4][v & 15], w2 = w * w, pz = __builtin_fmaf(k1, w2, k0); ez[v] = pz * w; } });
    sfor<0, 2>([&](auto Kc) { constexpr int v = val_at(G - 5, G0, RN, RD, decltype(Kc)::value);
      if constexpr (v >= 0) eo[v] = acc[BUF][v >> 4][v & 15] * ez[v]; });
    sfor<0, 2>([&](auto Kc) { constexpr int v = val_at(G - 3, G0, RN, RD, decltype(Kc)::value); if constexpr (v >= 0) ez[v] = __builtin_amdgcn_rcpf(ez[v]); });
    sfor<0, 2>([&](auto Kc) { constexpr int v = val_at(G - 2, G0, RN, RD, decltype(Kc)::value); if constexpr (v >= 0) ez[v] = __builtin_amdgcn_exp2f(ez[v]); });
    sfor<0, 2>([&](auto Kc) { constexpr int v = val_at(G - 5, G0, RN, RD, decltype(Kc)::value);
      if constexpr (v >= 0) {
        if constexpr (v & 1) hv[v >> 3][(v & 7) >> 1] = pk2(eo[v - 1], eo[v]);
        if constexpr ((v & 7) == 7) {
          if constexpr (uu == 1 || uu == 2) {
            __builtin_amdgcn_raw_buffer_store_b128(hv[v >> 3], rws, voH + (v >> 3) * 1024, hbase_own + uu * 16384, 16);
          } else *(LAS u32x4*)(size_t)(aHO + ((v >> 3) & 1) * 4096) = hv[v >> 3];
        }
      } });
  };
  auto aug = [&](auto Uc, auto Bc) {
    constexpr int uu = decltype(Uc)::value, BUF = decltype(Bc)::value;
#pragma unroll
    for (int t = 0; t < 2; ++t) {
      const bf16x8 af = *(LAS bf16x8*)(lds + L_AUG + (64 * uu + 32 * t + m) * 16);
      acc[BUF][t] = __builtin_amdgcn_mfma_f32_32x32x16_bf16(af, augB, acc[BUF][t], 0, 0, 0);
    }
  };
  auto g1stage = [&](auto Tc, auto Cc, auto Bc, auto NUc, auto Nc, auto Cr, auto&& work, auto&& syncnext) {
    constexpr int T = decltype(Tc)::value, c = decltype(Cc)::value, BUF = decltype(Bc)::value, NU = decltype(NUc)::value, NEXT = decltype(Nc)::value, kc = c1_kc(c);
    constexpr int NG = 8 * NU, PF = T < 9 ? 4 : PFP, G0 = T < 9 ? 0 : (8 * (T - 9)) % PF;
    constexpr int DN = NEXT >= 0 ? NEXT % 9 - c % 9 : 1;
    if constexpr (NEXT >= 0 && DN < 0) { b1en = b1e + DN * 8192; b1on = b1o + DN * 8192; asm volatile("" : "+v"(b1en), "+v"(b1on)); }
    __builtin_amdgcn_sched_barrier(0);
    sfor<0, NG>([&](auto Ic) {
      constexpr int I = decltype(Ic)::value, ks = I / (2 * NU), un = (I >> 1) % NU, tile = I & 1;
      const bf16x8 cur = pf[(G0 + I) % PF];
      if constexpr (I + PF < NG) pf[(G0 + I) % PF] = rd1(IC<I + PF>{}, NUc);
      else if constexpr (NEXT >= 0) pf[(G0 + I) % PF] = rd1n(IC<DN>{}, IC<I + PF - NG>{});
      acc[BUF + un][tile] = __builtin_amdgcn_mfma_f32_32x32x16_bf16(cur, xr[4 * kc + ks], (kc == 0 && ks == 0) ? zero16 : acc[BUF + un][tile], 0, 0, 0);
      __builtin_amdgcn_sched_barrier(0);
      if constexpr (I == NG - 1 && NEXT >= 0) adv1(IC<DN>{});
      if constexpr (I == 0 * NU) site(Tc, IC<0>{}, Cr);
      if constexpr (I == 2 * NU) site(Tc, IC<1>{}, Cr);
      if constexpr (I == 4 * NU) site(Tc, IC<2>{}, Cr);
      if constexpr (I == 6 * NU) site(Tc, IC<3>{}, Cr);
      if constexpr (I == NG - 1) {
        asm volatile("" : "+v"(acc[BUF][0]), "+v"(acc[BUF][1]));
        if constexpr (NU == 2) asm volatile("" : "+v"(acc[BUF + NU - 1][0]), "+v"(acc[BUF + NU - 1][1]));
        syncnext();
      }
      work(Ic, cur);
      __builtin_amdgcn_sched_barrier(0);
    });
  };
  auto dot2 = [](unsigned a, unsigned b, float c) { return __builtin_amdgcn_fdot2_f32_bf16(__builtin_bit_cast(bf16x2, a), __builtin_bit_cast(bf16x2, b), c, false); };
  const unsigned selw = ((lane & 15) == ((lane >> 4) & 1)) ? 0x3f803f80u : 0u;
  const bf16x8 selA = __builtin_bit_cast(bf16x8, u32x4{selw, selw, selw, selw});
  auto xfinish = [&]() {
    auto both = [](float v) { const unsigned u = __builtin_bit_cast(unsigned, v); const auto r = __builtin_amdgcn_permlane32_swap(u, u, false, false); return asf(r[0]) + asf(r[1]); };
    const float t2 = both((st2[0] + st2[1]) + (st2[2] + st2[3]));
    const int l = flane();
    if (l < 16) { *(LAS float*)(lds + L_ST + role * 512 + (32 * rg + l) * 4) = st1[0]; *(LAS float*)(lds + L_ST + role * 512 + (32 * rg + 16 + l) * 4) = st1[1]; }
    if (l < 32) *(LAS float*)(lds + L_ST + 1024 + role * 512 + (32 * rg + l) * 4) = t2;
    asm volatile("" : "+v"(csv[0]), "+v"(csv[1]), "+v"(csv[2]), "+v"(csv[3]), "+v"(csv[4]), "+v"(csv[5]), "+v"(csv[6]), "+v"(csv[7]));
    if (wave < 6) {
      const float cs = ((asf(csv[0]) + asf(csv[1])) + (asf(csv[2]) + asf(csv[3]))) + ((asf(csv[4]) + asf(csv[5])) + (asf(csv[6]) + asf(csv[7]))), ch = rb(cs);
      const int f = 64 * wave + l;
      *(LAS u32x4*)(lds + L_AUG + f * 16) = u32x4{pk2(ch, cs - ch), *(LAS u16*)(lds + L_B1RAW + f * 2), 0u, 0u};
    }
    lgkm0();
  };
  auto takestats = [&]() {
    const LAS float* q = (const LAS float*)(lds + L_ST) + 32 * rg + m;
    const float s1 = q[0] + q[128], s2 = q[256] + q[384];
    const float mean = s1 * (1.0f / D_), var = __builtin_fmaxf(__builtin_fmaf(-mean, mean, s2 * (1.0f / D_)), 0.f);
    mean_f = rb(mean); sd = rb(__builtin_amdgcn_sqrtf(rb(rb(var) + 1e-5f)));
    const float r = __builtin_amdgcn_rcpf(sd); k0 = -2.302208198f * r; k1 = -0.1029432397f * r * r * r;
    const u32x4 bb = {hh ? 0u : (pk2(-mean_f, -mean_f)), hh ? 0u : (pk2(sd, 0.f)), 0u, 0u};
    augB = __builtin_bit_cast(bf16x8, bb);
  };
  auto syncX = [&](auto Tc, auto Cr) {
    constexpr int T = decltype(Tc)::value; constexpr bool C = decltype(Cr)::value;
    if constexpr (T == 8) {
      if (wave == 0) { waitv<after(5, 8, C)>(); asm volatile("" : "+v"(pollv)); const int l = flane(); if (!wall(!actFA(l) || flag_ok(pollv, base, 1))) spin_flags(p.ws, rws, voFA(l), actFA(l), base, 1); }
    }
    waitv<waitn(T, C)>();
    bar();
    if constexpr (T == 0) { base = take_epoch(p.ws, epv); STAMP(1); }
    if constexpr (T == 6) { if (wave == 0) ld8_sc1(pollv, voFA(flane()), rws, 0); }
    if constexpr (T == 8) {
      const unsigned vo = (384 * j + 64 * (wave < 6 ? wave : 0) + flane()) * 4;
#pragma unroll
      for (int r = 0; r < 8; ++r) ld4_sc1(csv[r], vo, rws, WS_CS + r * F_ * 4);
    }
  };
  auto xphase = [&](auto Cr, auto&& sync9) {
    constexpr bool C = decltype(Cr)::value;
    constexpr int NU = C ? 2 : 1;
    syncX(IC<0>{}, Cr);
    sfor<0, 9>([&](auto Tc) {
      constexpr int T = decltype(Tc)::value;
      constexpr int cc = 3 * T + (C ? 1 : 0);
      if constexpr (T > 0) adv1(IC<cc % 9 - (cc - 3) % 9>{});
      sfor<0, 4>([&](auto Kc) { constexpr int ks = decltype(Kc)::value;
        xr[4 * T + ks] = *(LAS bf16x8*)(size_t)((ax ^ (ks << 5)) + x_pos(T) * 16384); });
      sfor<0, PF>([&](auto Ic) { pf[decltype(Ic)::value] = rd1(Ic, IC<NU>{}); });
      g1stage(Tc, IC<cc>{}, IC<0>{}, IC<NU>{}, IC<-1>{}, Cr, [&](auto Ic, const bf16x8&) {
        constexpr int I = decltype(Ic)::value, ks = I / (2 * NU);
        if constexpr (I % (2 * NU) == 0 && ((ks >> 1) == (C ? 1 : 0))) {
          st1 = __builtin_amdgcn_mfma_f32_16x16x32_bf16(selA, xr[4 * T + ks], st1, 0, 0, 0);
          const u32x4 x = __builtin_bit_cast(u32x4, xr[4 * T + ks]);
#pragma unroll
          for (int e = 0; e < 4; ++e) st2[e] = dot2(x[e], x[e], st2[e]);
        }
      }, [&]() { if constexpr (T < 8) syncX(IC<T + 1>{}, Cr); else sync9(); });
    });
  };
  if (role == 0) {
    u32x4 zq[3];
    auto syncP = [&](auto Tc) {
      constexpr int T = decltype(Tc)::value;
      __builtin_amdgcn_sched_barrier(0);
      if constexpr (T == 9) { waitv<cnt(8, false)>(); xfinish(); }
      waitv<(T == TZ2 ? 0 : waitn(T, false))>();
      if constexpr (T > TP && T <= TP + 5) lgkm0();
      if constexpr (T - 1 < TP && ho_written(T - 1)) asm volatile("s_waitcnt lgkmcnt(6)" ::: "memory");
      bar();
      if constexpr (T == 9) takestats();
    };
    xphase(std::false_type{}, [&]() { syncP(IC<9>{}); });
    mkW2c(flane());
    STAMP(2);
    sfor<9, NST>([&](auto Tc) {
      constexpr int T = decltype(Tc)::value;
      if constexpr (T < TP) {
        constexpr int u = 3 + (T - 9) / 9, kc = (T - 9) % 9, PU = u == 3 ? 0 : u - 1;
        constexpr int UB = u & 1, EB = UB ^ 1;
        if constexpr (T == 9) { adv1(IC<27 % 9 - 24 % 9>{}); sfor<0, PFP>([&](auto Ic) { pf[decltype(Ic)::value] = rd1(Ic, IC<1>{}); }); }
        if constexpr (kc == 0) aug(IC<PU>{}, IC<EB>{});
        g1stage(Tc, IC<T + 18>{}, IC<UB>{}, IC<1>{}, IC<(T + 1 < TP ? T + 19 : -1)>{}, std::false_type{}, [&](auto Ic, const bf16x8&) {
          epigap(IC<PU>{}, IC<EB>{}, IC<8 * kc + decltype(Ic)::value>{}, IC<1>{}, IC<2>{}, IC<1>{}); }, [&]() { syncP(IC<T + 1>{}); });
      } else {
        if constexpr (T == TP) { STAMP(3); aug(IC<5>{}, IC<1>{}); }
        allsites(Tc, std::false_type{});
        if constexpr (T == TZ1) {
          const int l = flane();
#pragma unroll
          for (int ii = 0; ii < 3; ++ii) { const int pc = 3 * rg + ii;
            ld16_sc1_s(zq[ii], (4 * pc + (l >> 4)) * 4096 + (l & 15) * 16, rws, WS_ZT + (192 * jn + 48 * s) * 4096 + (128 * i) * 2); }
#pragma unroll
          for (int ii = 0; ii < 3; ++ii) { const unsigned L = (3 * rg + ii) * 1024 + l * 16, row = L / 96, col = L - 96 * row;
            dma16(rfl(ldsb + L_XF + (3 * rg + ii) * 1024), row * 1152 + col, rx, rfl((128 * i) * 1152 + (192 * jn + 48 * s) * 2)); }
        }
        if constexpr (T == TZ2) {
          asm volatile("" : "+v"(zq[0]), "+v"(zq[1]), "+v"(zq[2]));
          const int l = flane();
#pragma unroll
          for (int ii = 0; ii < 3; ++ii) *(LAS u32x4*)(lds + L_Z + (3 * rg + ii) * 1024 + l * 16) = zq[ii];
          lgkm0();
        }
        if constexpr (T < TP + 5) sfor<0, 8>([&](auto Ic) { epigap(IC<5>{}, IC<1>{}, IC<8 * (T - TP) + decltype(Ic)::value>{}, IC<1>{}, IC<1>{}, IC<1>{}); });
        if constexpr (T + 1 < NST) syncP(IC<T + 1>{});
      }
    });
    return;
  }
  f32x16 acc2[6];
  u32x4 hring[3][4], hown[2][4];
  unsigned b2e[2], b2o[2];
  auto base2 = [&](auto Bc) { constexpr int b = decltype(Bc)::value;
    unsigned e = a2e + w2_buf(b) * 24576, o = a2o + w2_buf(b) * 24576; asm volatile("" : "+v"(e), "+v"(o)); b2e[b & 1] = e; b2o[b & 1] = o; };
  auto rd2 = [&](auto Bc, auto Gc) -> bf16x8 { constexpr int b = decltype(Bc)::value, G = decltype(Gc)::value, ks = g_ks(b, G), tn = g_tn(b, G);
    return trr(((tn & 1) ? b2o[b & 1] : b2e[b & 1]) + ks * 16 * 384 + 32 * (tn >> 1), 8 * 384); };
  const unsigned hbA = WS_H + (unsigned)(i * 12 + jA) * 98304u, hbB = WS_H + (unsigned)(i * 12 + jB) * 98304u;
  const unsigned sH[3] = {hbase_own, hbA, hbB};
  auto issueH = [&](auto Tc, auto Gc) {
    constexpr int T = decltype(Tc)::value, G = decltype(Gc)::value;
    sfor<0, 2>([&](auto Hc) {
      constexpr int half = decltype(Hc)::value, b = h_at(T - half);
      if constexpr (b >= 0) {
        constexpr int r = kRing[b], k = 2 * half + G;
        ld16i_sc1<bunit(b) * 16384 + k * 1024>(hring[r][k], voH, rws, sH[bsel(b)]);
      }
    });
  };
  u32x2 progv = {0u, 0u};
  unsigned proga = WS_PROG + (i * 12 + j) * FL_STRIDE;
  auto voPR = [&](int l) -> unsigned { return WS_PROG + (i * 12 + (l == 0 ? jA : jB)) * FL_STRIDE; };
  auto actFZ = [&](int l) { return ((192 * jn + 48 * s) / 9 + l) <= (192 * jn + 48 * s + 47) / 9; };
  auto voFZ = [&](int l) -> unsigned { return actFZ(l) ? WS_FZ + ((192 * jn + 48 * s) / 9 + l) * FL_STRIDE : WS_FZ; };
  u32x2 pollz = {0u, 0u};
  auto syncC = [&](auto Tc) {
    constexpr int T = decltype(Tc)::value, b = blk_of(T);
    __builtin_amdgcn_sched_barrier(0);
    if constexpr (T == 9) { waitv<cnt(8, true)>(); xfinish(); }
    if constexpr (chk_at(T) >= 0) {
      constexpr int bb = chk_at(T);
      if (wave == 4) { waitv<after(T - 3, T, true)>(); asm volatile("" : "+v"(pollv)); const int l = flane();
        if (!wall(l >= 2 || flag_ok(pollv, base, needOf(bunit(bb))))) spin_flags(p.ws, rws, voPR(l), l < 2, base, needOf(bunit(bb)));
        if constexpr (bb == NB - 2) { asm volatile("" : "+v"(pollz)); if (!wall(!actFZ(l) || flag_ok(pollz, base, 1))) spin_flags(p.ws, rws, voFZ(l), actFZ(l), base, 1); } }
    }
    if constexpr (pubK(T) > 0) waitv<after(T - 5, T, true)>();
    waitv<waitn(T, true)>();
    if constexpr (b >= 0 && bstart(b < 0 ? 0 : b) == T && kRing[b < 0 ? 0 : b] >= 0) { constexpr int r = kRing[b < 0 ? 0 : b]; asm volatile("" : "+v"(hring[r][0]), "+v"(hring[r][1]), "+v"(hring[r][2]), "+v"(hring[r][3])); }
    bar();
    if constexpr (pubK(T) > 0) {
      progv.x += 1;
      if (wave == 4) asm volatile("s_mov_b64 exec, 1\n\tbuffer_store_dwordx2 %0, %1, %2, 0 offen sc1\n\ts_mov_b64 exec, -1" :: "v"(progv), "v"(proga), "s"(rws) : "memory");
    }
    if constexpr (T == 9) takestats();
    if constexpr (T == PRIOWIN) __builtin_amdgcn_s_setprio(0);
    if constexpr (chk_at(T + 2) >= 0) {
      if (wave == 4) { const int l = flane(); ld8_sc1(pollv, voPR(l), rws, 0); if constexpr (chk_at(T + 2) == NB - 2) ld8_sc1(pollz, voFZ(l), rws, 0); }
    }
    constexpr int cu = cr_unit(T), ck = cr_k(T);
    if constexpr (cu >= 0) hown[ownset(cu)][ck] = *(LAS u32x4*)(size_t)(aHO + (ck & 1) * 4096);
  };
  auto chunk_store = [&](auto Tc) {
    constexpr int T = decltype(Tc)::value, cu = cr_unit(T), ck = cr_k(T);
    if constexpr (cu >= 0) __builtin_amdgcn_raw_buffer_store_b128(hown[ownset(cu)][ck], rws, voH + ck * 1024, hbase_own + cu * 16384, 16);
  };
  const unsigned pbase = WS_PART + (unsigned)(i * 3 + jn) * (16 * 12288) + rg * 3072;
  u32x4 pkeep[9];
  auto pstore = [&](auto Cc, auto Nc) {
    constexpr int c = decltype(Cc)::value, n = decltype(Nc)::value;
    const f32x16 &a = acc2[2 * c], &d = acc2[2 * c + 1];
    const bool up = n >= s;
    auto pick = [&](const f32x16& v, int e) { return up ? v[4 * (n + 1) + e] : v[4 * n + e]; };
    const u32x4 va = {pk2(pick(a, 0), pick(a, 1)), pk2(pick(a, 2), pick(a, 3)), pk2(pick(d, 0), pick(d, 1)), pk2(pick(d, 2), pick(d, 3))};
    pkeep[3 * c + n] = va;
    __builtin_amdgcn_raw_buffer_store_b128(pkeep[3 * c + n], rws, voH + c * 1024, pbase - rg * 4096 + ((n + (up ? 1 : 0)) * 4 + s) * 12288, 16);
  };
  auto gelu2 = [&](auto Tc, auto Ic) {
    constexpr int T = decltype(Tc)::value, G = 8 * (T - TG2) + decltype(Ic)::value;
    if constexpr (T >= TG2 && G <= 68) {
      epigap(IC<2>{}, IC<1>{}, IC<G>{}, IC<1>{}, IC<2>{}, IC<1>{});
      if constexpr (G >= 20 && (G - 20) % 16 == 0) hown[ownset(2)][(G - 20) / 16] = hv[(G - 20) / 16];
    }
  };
  mkW2c(lane);
  xphase(std::true_type{}, [&]() { syncC(IC<9>{}); });
  progv = u32x2{(unsigned)base, (unsigned)(base >> 32)};
  if constexpr (PRIOWIN > 9) __builtin_amdgcn_s_setprio(1);
  STAMP(2);
  sfor<9, NST>([&](auto Tc) {
    constexpr int T = decltype(Tc)::value, b = blk_of(T), part = b >= 0 ? T - bstart(b) : 0;
    if constexpr (T < 12) {
      if constexpr (T == 9) { aug(IC<1>{}, IC<0>{}); aug(IC<2>{}, IC<1>{}); }
      allsites(Tc, std::true_type{});
      sfor<0, 8>([&](auto Ic) { epigap(IC<1>{}, IC<0>{}, IC<8 * (T - 9) + decltype(Ic)::value>{}, IC<1>{}, IC<4>{}, IC<7>{}); });
      if constexpr (T == 11) {
#pragma unroll
        for (int k = 0; k < 4; ++k) hown[ownset(1)][k] = hv[k];
        base2(IC<0>{}); sfor<0, PF>([&](auto Ic) { pf[decltype(Ic)::value] = rd2(IC<0>{}, Ic); });
      }
      syncC(IC<T + 1>{});
    } else if constexpr (b < 0) {
      allsites(Tc, std::true_type{});
      issueH(Tc, IC<0>{}); issueH(Tc, IC<1>{});
      chunk_store(Tc);
      sfor<0, 8>([&](auto Ic) { gelu2(Tc, Ic); });
      if constexpr (blk_of(T + 1) >= 0) { base2(IC<blk_of(T + 1)>{}); sfor<0, PF>([&](auto Ic) { pf[decltype(Ic)::value] = rd2(IC<blk_of(T + 1)>{}, Ic); }); }
      syncC(IC<T + 1>{});
    } else {
      constexpr bool nextblk = part == 2 && blk_of(T + 1) >= 0;
      if constexpr (nextblk) base2(IC<b + 1>{});
      __builtin_amdgcn_sched_barrier(0);
      sfor<0, 8>([&](auto Ic) {
        constexpr int I = decltype(Ic)::value, G = 8 * part + I, ks = g_ks(b, G), tn = g_tn(b, G);
        const bf16x8 cur = pf[I % PF];
        if constexpr (G + PF < 24) pf[I % PF] = rd2(IC<b>{}, IC<G + PF>{});
        else if constexpr (nextblk) pf[I % PF] = rd2(IC<b + 1>{}, IC<G + PF - 24>{});
        u32x4 hb;
        if constexpr (bsel(b) == 0) hb = hown[ownset(bunit(b))][ks]; else hb = hring[kRing[b] < 0 ? 0 : kRing[b]][ks];
        acc2[tn] = __builtin_amdgcn_mfma_f32_32x32x16_bf16(cur, __builtin_bit_cast(bf16x8, hb), (b == 0 && ks == 0) ? zero16 : acc2[tn], 0, 0, 0);
        __builtin_amdgcn_sched_barrier(0);
        if constexpr (I == 0) site(Tc, IC<0>{}, std::true_type{});
        if constexpr (I == 2) site(Tc, IC<1>{}, std::true_type{});
        if constexpr (I == 1) issueH(Tc, IC<0>{});
        if constexpr (I == 3) issueH(Tc, IC<1>{});
        if constexpr (I == 4) site(Tc, IC<2>{}, std::true_type{});
        if constexpr (I == 5) chunk_store(Tc);
        if constexpr (I == 6) site(Tc, IC<3>{}, std::true_type{});
        if constexpr (b == NB - 1 && part >= 1 && I >= 2 && I <= 4) pstore(IC<part - 1>{}, IC<I - 2>{});
        if constexpr (I == 7) {
          asm volatile("" : "+v"(acc2[0]), "+v"(acc2[1]), "+v"(acc2[2]), "+v"(acc2[3]), "+v"(acc2[4]), "+v"(acc2[5]));
          if constexpr (T + 1 < NST) syncC(IC<T + 1>{});
        }
        gelu2(Tc, Ic);
        __builtin_amdgcn_sched_barrier(0);
      });
    }
  });
  asm volatile("" :: "v"(progv), "v"(proga));
  STAMP(4);
  TLDUMP();
  const int tl = flane(), tm = tl & 31, thh = tl >> 5;
  const int pos = 128 * i + 32 * rg + tm;
  pstore(IC<2>{}, IC<0>{}); pstore(IC<2>{}, IC<1>{}); pstore(IC<2>{}, IC<2>{});
  float pre[24];
  {
    float own[24];
    sfor<0, 4>([&](auto Gc) { constexpr int g = decltype(Gc)::value;
      if (g == s) {
#pragma unroll
        for (int tn = 0; tn < 6; ++tn)
#pragma unroll
          for (int e = 0; e < 4; ++e) own[4 * tn + e] = acc2[tn][4 * g + e];
      } });
    const float rstd = __builtin_amdgcn_rcpf(sd);
#pragma unroll
    for (int tn = 0; tn < 6; ++tn) {
      const int cl = 8 * tn + 4 * thh;
      const s16x4 zt = __builtin_amdgcn_ds_read_tr16_b64_v4i16((LAS s16x4*)(lds + L_Z + (cl + ((tl & 15) >> 2)) * 256 + (32 * rg + 16 * ((tl >> 4) & 1) + 4 * (tl & 3)) * 2));
      const u32x2 bv = *(LAS u32x2*)(lds + L_B2RAW + (48 * s + cl) * 2);
      const u32x2 xv = *(LAS u32x2*)(lds + L_XF + (32 * rg + tm) * 96 + cl * 2);
#pragma unroll
      for (int e = 0; e < 4; ++e) {
        const float xe = (e & 1) ? bhi(xv[e >> 1]) : blo(xv[e >> 1]);
        const float be_ = (e & 1) ? bhi(bv[e >> 1]) : blo(bv[e >> 1]);
        const float xn = rb((xe - mean_f) * rstd);
        pre[4 * tn + e] = __builtin_fmaf(xn, b2f((u16)zt[e]), xe) + (own[4 * tn + e] + be_);
      }
    }
  }
  asm volatile("s_waitcnt vmcnt(0)" :: "v"(pkeep[0]), "v"(pkeep[1]), "v"(pkeep[2]), "v"(pkeep[3]), "v"(pkeep[4]), "v"(pkeep[5]), "v"(pkeep[6]), "v"(pkeep[7]), "v"(pkeep[8]) : "memory");
  if (tl == 0) flag_store(p.ws, WS_PFL + (((i * 3 + jn) * 4 + s) * 4 + rg) * FL_STRIDE, base + 1);
  STAMP(5);
  spin_flags(p.ws, rws, WS_PFL + (((i * 3 + jn) * 4 + (tl & 3)) * 4 + rg) * FL_STRIDE, tl < 4 && tl != s, base, 1);
  STAMP(6);
  u32x4 pa[3][3];
#pragma unroll
  for (int o = 0; o < 3; ++o) { const int so = o + (o >= s ? 1 : 0);
#pragma unroll
    for (int c = 0; c < 3; ++c) ld16_sc1_s(pa[o][c], tl * 16 + c * 1024, rws, pbase + (s * 4 + so) * 12288); }
  asm volatile("s_waitcnt vmcnt(0)" : "+v"(pa[0][0]), "+v"(pa[0][1]), "+v"(pa[0][2]), "+v"(pa[1][0]), "+v"(pa[1][1]), "+v"(pa[1][2]), "+v"(pa[2][0]), "+v"(pa[2][1]), "+v"(pa[2][2]) :: "memory");
  STAMP(8);
#pragma unroll
  for (int tn = 0; tn < 6; ++tn) {
    float o[4];
#pragma unroll
    for (int e = 0; e < 4; ++e) {
      float sum = pre[4 * tn + e];
#pragma unroll
      for (int oo = 0; oo < 3; ++oo) { const unsigned wd = pa[oo][tn >> 1][2 * (tn & 1) + (e >> 1)]; sum += (e & 1) ? bhi(wd) : blo(wd); }
      o[e] = sum;
    }
    const u32x2 ov = {pk2(o[0], o[1]), pk2(o[2], o[3])};
    *(GAS u32x2*)(p.out + (size_t)pos * D_ + 192 * jn + 48 * s + 8 * tn + 4 * thh) = ov;
  }
  STAMP(7);
}

constexpr unsigned A_CH = 12928;
constexpr unsigned A_H0 = 32, A_H1 = A_H0 + 4096 + 64, A_X = A_H1 + 4096 + 512  ;
static_assert(A_X + 4096 <= A_CH && A_X % 16 == 0, "channel image");
typedef unsigned u32x4a4 __attribute__((ext_vector_type(4), aligned(4)));
typedef float f32x4v __attribute__((ext_vector_type(4)));

constexpr unsigned A_STG = 9 * A_CH;
constexpr unsigned A_CSP = A_STG;
constexpr unsigned A_RK = 0, A_RV = 40960;
constexpr unsigned A_JUNK = A_STG + 288 * 144;
static_assert(A_JUNK + 4096 <= 163840, "aux LDS");

DI void aux_role(const Params& p, LAS char* lds, const int a, const int al, const int cset, u32x2 epv) {
  u64 base;
  const int tid = threadIdx.x, lane = tid & 63, wave = rfl(tid >> 6);
  const rsrc_t rws = mkrsrc(p.ws, WS_END);
  const unsigned ldsb = (unsigned)(size_t)lds;
  STAMP(0);
  constexpr unsigned OOB = 0x7fffff00u;
  const rsrc_t rw1 = mkrsrc(p.w1, D_ * F_ * 2);
  u32x4 wv[11];
  const int r7 = tid / 72, c72 = tid - 72 * r7;
#pragma unroll
  for (int k = 0; k < 11; ++k) {
    const int rl = r7 + 7 * k;
    wv[k] = __builtin_amdgcn_raw_buffer_load_b128(rw1, (r7 < 7 && rl < 72) ? (unsigned)((72 * al + rl) * 9216 + (576 * cset) * 2 + c72 * 16) : OOB, 0, 0);
  }
  const int pt = a >> 1, hf = a & 1;
  const rsrc_t rk = mkrsrc(p.k, S_ * D_ * 2), rv = mkrsrc(p.v, S_ * D_ * 2), rs = mkrsrc(p.shk, D_ * 8), rh = mkrsrc(p.ssk, D_ * S_ * 2);
#pragma unroll
  for (int n = 0; n < 5; ++n) {
    const unsigned L = (wave + 8 * n) * 1024 + lane * 16, row = L / 576, col = L - 576 * row;
    const int sr = 64 * pt - 3 + (int)row;
    dma16(ldsb + A_RK + rfl(wave + 8 * n) * 1024, (row < 67 && sr >= 0) ? (unsigned)(sr * 1152) + col : OOB, rk, 576 * hf);
    dma16(ldsb + A_RV + rfl(wave + 8 * n) * 1024, row < 64 ? (unsigned)((64 * pt + row) * 1152) + col : OOB, rv, 576 * hf);
  }
  u32x4 sh[3];
#pragma unroll
  for (int n = 0; n < 3; ++n) { const int it = tid + 512 * n, cp = it % 144; sh[n] = __builtin_amdgcn_raw_buffer_load_b128(rs, (288 * hf + 2 * cp) * 8, 0, 0); }
  {
    float cs[8] = {0.f, 0.f, 0.f, 0.f, 0.f, 0.f, 0.f, 0.f};
#pragma unroll
    for (int k = 0; k < 11; ++k)
#pragma unroll
      for (int e = 0; e < 4; ++e) { cs[2 * e] += blo(wv[k][e]); cs[2 * e + 1] += bhi(wv[k][e]); }
    if (r7 < 7) { LAS char* dst = lds + A_CSP + (r7 * 576 + c72 * 8) * 4;
      *(LAS f32x4*)dst = f32x4{cs[0], cs[1], cs[2], cs[3]}; *(LAS f32x4*)(dst + 16) = f32x4{cs[4], cs[5], cs[6], cs[7]}; }
    base = take_epoch(p.ws, epv);
    sbar();
    if (tid < 144) {
      f32x4 t = *(LAS f32x4*)(lds + A_CSP + tid * 16);
#pragma unroll
      for (int r = 1; r < 7; ++r) { const f32x4 u = *(LAS f32x4*)(lds + A_CSP + r * 2304 + tid * 16); t = f32x4{t[0] + u[0], t[1] + u[1], t[2] + u[2], t[3] + u[3]}; }
      __builtin_amdgcn_raw_buffer_store_b128(__builtin_bit_cast(u32x4, t), rws, WS_CS + (al * F_ + 576 * cset) * 4 + tid * 16, 0, 16);
    }
  }
  STAMP(6);
  asm volatile("s_waitcnt vmcnt(0) lgkmcnt(0)\n\ts_barrier" : "+v"(sh[0]), "+v"(sh[1]), "+v"(sh[2]) :: "memory");
  if (tid == 0) flag_store(p.ws, WS_FA + (cset * 8 + al) * FL_STRIDE, base + 1);
  STAMP(2);
  unsigned touch[2];
  {
    const rsrc_t rw2 = mkrsrc(p.w2, F_ * D_ * 2);
#pragma unroll
    for (int k = 0; k < 2; ++k) { const int ln = tid + 512 * k;
      asm volatile("buffer_load_dword %0, %1, %2, %3 offen" : "=v"(touch[k]) : "v"(ln < 648 ? (unsigned)(ln * 128) : OOB), "s"(rw2), "s"((unsigned)((576 * cset + 72 * al) * 1152)) : "memory"); }
  }
  u32x4 kvo[3][2];
#pragma unroll
  for (int n = 0; n < 3; ++n) {
    const int it = (tid + 512 * n) < 1152 ? tid + 512 * n : tid, o = it / 144, cp = it - 144 * o;
    unsigned kk[11], vv[8];
#pragma unroll
    for (int r = 0; r < 11; ++r) kk[r] = *(LAS unsigned*)(lds + A_RK + (8 * o + r) * 576 + cp * 4);
#pragma unroll
    for (int r = 0; r < 8; ++r) vv[r] = *(LAS unsigned*)(lds + A_RV + (8 * o + r) * 576 + cp * 4);
    float o0[8], o1[8];
#pragma unroll
    for (int r = 0; r < 8; ++r) {
      float a0 = blo(kk[r]) * bhi(sh[n][1]); a0 = __builtin_fmaf(blo(kk[r + 1]), blo(sh[n][1]), a0); a0 = __builtin_fmaf(blo(kk[r + 2]), bhi(sh[n][0]), a0); a0 = __builtin_fmaf(blo(kk[r + 3]), blo(sh[n][0]), a0);
      float a1 = bhi(kk[r]) * bhi(sh[n][3]); a1 = __builtin_fmaf(bhi(kk[r + 1]), blo(sh[n][3]), a1); a1 = __builtin_fmaf(bhi(kk[r + 2]), bhi(sh[n][2]), a1); a1 = __builtin_fmaf(bhi(kk[r + 3]), blo(sh[n][2]), a1);
      o0[r] = a0 * blo(vv[r]); o1[r] = a1 * bhi(vv[r]);
    }
    kvo[n][0] = u32x4{pk2(o0[7], o0[6]), pk2(o0[5], o0[4]), pk2(o0[3], o0[2]), pk2(o0[1], o0[0])};
    kvo[n][1] = u32x4{pk2(o1[7], o1[6]), pk2(o1[5], o1[4]), pk2(o1[3], o1[2]), pk2(o1[1], o1[0])};
  }
#pragma unroll
  for (int n = 0; n < 3; ++n) {
    const int it = tid + 512 * n, o = it / 144, cp = it - 144 * o;
    if (it < 1152) { LAS char* dst = lds + A_STG + (2 * cp) * 144 + o * 16; *(LAS u32x4*)dst = kvo[n][0]; *(LAS u32x4*)(dst + 144) = kvo[n][1]; }
  }
  STAMP(8);
  u32x4 hA[5]; unsigned hB[5];
#pragma unroll
  for (int n = 0; n < 5; ++n) {
    const int un = wave + 8 * n, c = un >> 2, i = un & 3;
    const unsigned go = un < 36 ? (unsigned)((9 * a + c) * 4096 + i * 1024 + lane * 16) : OOB;
    asm volatile("buffer_load_dwordx4 %0, %2, %4, 0 offen\n\tbuffer_load_dword %1, %3, %4, 0 offen" : "=&v"(hA[n]), "=&v"(hB[n]) : "v"(go), "v"(un < 36 ? go + 16 : OOB), "s"(rh) : "memory");
  }
  sbar();
  if (tid < 9 * 35) { const int c = tid / 35, i = tid - 35 * c;
    const unsigned off = c * A_CH + (i < 2 ? i * 16 : i < 3 ? A_H1 - 32 : A_X - 512 + (i - 3) * 16);
    *(LAS u32x4*)(lds + off) = u32x4{0u, 0u, 0u, 0u}; }
#pragma unroll
  for (int n = 0; n < 5; ++n) {
    const int gch = wave + 8 * n;
    if (gch < 36) {
      const int ch = 8 * gch + (lane >> 3), o = lane & 7;
      const u32x4 v = *(LAS u32x4*)(lds + A_STG + ch * 144 + o * 16);
      __builtin_amdgcn_raw_buffer_store_b128(v, rws, WS_KVT + (288 * hf + ch) * 4096 + (64 * pt + 8 * o) * 2, 0, 16);
    }
  }
  asm volatile("s_waitcnt vmcnt(4)" : "+v"(hA[0]), "+v"(hA[1]), "+v"(hA[2]), "+v"(hA[3]), "+v"(hA[4]), "+v"(hB[0]), "+v"(hB[1]), "+v"(hB[2]), "+v"(hB[3]), "+v"(hB[4]) :: "memory");
  asm volatile("" : "+v"(touch[0]), "+v"(touch[1]));
#pragma unroll
  for (int n = 0; n < 5; ++n) {
    const int un = wave + 8 * n, c = un >> 2, i = un & 3;
    if (un < 36) {
      const u32x4 A = hA[n];
      *(LAS u32x4*)(lds + c * A_CH + A_H0 + i * 1024 + lane * 16) = A;
      *(LAS u32x4*)(lds + c * A_CH + A_H1 + i * 1024 + lane * 16) =
          u32x4{__builtin_amdgcn_alignbit(A[1], A[0], 16), __builtin_amdgcn_alignbit(A[2], A[1], 16), __builtin_amdgcn_alignbit(A[3], A[2], 16), __builtin_amdgcn_alignbit(hB[n], A[3], 16)};
      if (i == 0 && lane == 0) *(LAS u32x4*)(lds + c * A_CH + A_H1 - 16) = u32x4{0u, 0u, 0u, A[0] << 16};
    }
  }
  STAMP(5);
  publish_flag(p.ws, WS_FB + a * FL_STRIDE, base + 1);
  STAMP(1);
  {
    const int hfc = (9 * a) >= 288 ? 1 : 0;
    if (wave == 0) {
      spin_flags(p.ws, rws, WS_FB + (2 * (lane & 31) + hfc) * FL_STRIDE, lane < 32, base, 1);
      __builtin_amdgcn_fence(__ATOMIC_ACQUIRE, "agent");
      asm volatile("s_waitcnt vmcnt(0)" ::: "memory");
    }
    sbar();
#pragma unroll
    for (int n = 0; n < 5; ++n) {
      const int un = wave + 8 * n, c = un >> 2, i = un & 3;
      if (un < 36) dma16(rfl(ldsb + c * A_CH + A_X + i * 1024), lane * 16, rws, rfl(WS_KVT + (9 * a + c) * 4096 + i * 1024));
    }
    asm volatile("s_waitcnt vmcnt(0) lgkmcnt(0)\n\ts_barrier" ::: "memory");
  }
  STAMP(3);
  const int b = lane & 15, g = lane >> 4;
  const int odd = (b + 1) & 1;
  const unsigned aH = (odd ? A_H1 : A_H0) + 2 * (9 + b - 8 * g - odd);
  const unsigned aX = A_X + 32 * b - 32 + 16 * g;
  auto rdH = [&](unsigned chb, int mp) -> bf16x8 { return __builtin_bit_cast(bf16x8, (u32x4)*(LAS u32x4a4*)(lds + chb + aH + 64 * mp)); };
  auto rdX = [&](unsigned chb, int n, int kappa) -> bf16x8 { return *(LAS bf16x8*)(lds + chb + aX + 512 * n - 64 * kappa); };
  const float dd0 = b2f(p.ssd[9 * a + wave]), dd8 = b2f(p.ssd[9 * a + 8]);
  auto finish = [&](int c, int t, f32x4v y) {
    const u32x2 kvr = *(LAS u32x2*)(lds + c * A_CH + A_X + (32 * t + 2 * b + (g >> 1)) * 16 + ((g & 1) ? 0 : 8));
    const float dd = c == 8 ? dd8 : dd0;
    const float z0 = rb(y[0]) + rb(bhi(kvr[1]) * dd), z1 = rb(y[1]) + rb(blo(kvr[1]) * dd), z2 = rb(y[2]) + rb(bhi(kvr[0]) * dd), z3 = rb(y[3]) + rb(blo(kvr[0]) * dd);
    __builtin_amdgcn_raw_buffer_store_b64(u32x2{pk2(z0, z1), pk2(z2, z3)}, rws, WS_ZT + (9 * a + c) * 4096 + (256 * t + 16 * b + 4 * g) * 2, 0, 16);
  };
  {
    f32x4v acc[8];
#pragma unroll
    for (int t = 0; t < 8; ++t) acc[t] = f32x4v{0.f, 0.f, 0.f, 0.f};
    unsigned bH[4], bX = ldsb + wave * A_CH + aX;
#pragma unroll
    for (int q4 = 0; q4 < 4; ++q4) bH[q4] = ldsb + wave * A_CH + aH + 1024 * q4;
    auto ldH = [&](int m) -> bf16x8 { return __builtin_bit_cast(bf16x8, (u32x4)*(LAS u32x4a4*)(size_t)(bH[m >> 1] + 512 * (m & 1))); };
    auto ldX = [&](int n) -> bf16x8 { return *(LAS bf16x8*)(size_t)(bX + 512 * n); };
    auto step = [&]() {
#pragma unroll
      for (int q4 = 0; q4 < 4; ++q4) bH[q4] += 64;
      bX -= 64;
      asm volatile("" : "+v"(bH[0]), "+v"(bH[1]), "+v"(bH[2]), "+v"(bH[3]), "+v"(bX));
    };
    bf16x8 H[2][8], X[2][8];
    asm volatile("" : "+v"(bH[0]), "+v"(bH[1]), "+v"(bH[2]), "+v"(bH[3]), "+v"(bX));
#pragma unroll
    for (int m = 0; m < 8; ++m) { H[0][m] = ldH(m); X[0][m] = ldX(m); }
#pragma unroll 1
    for (int kp = 0; kp < 4; ++kp) {
#pragma unroll
      for (int hb = 0; hb < 2; ++hb) {
        step();
        __builtin_amdgcn_sched_barrier(0);
#pragma unroll
        for (int m = 0; m < 8; ++m) {
          H[hb ^ 1][m] = ldH(m); X[hb ^ 1][m] = ldX(m);
#pragma unroll
          for (int t = m; t < 8; ++t) acc[t] = __builtin_amdgcn_mfma_f32_16x16x32_bf16(H[hb][m], X[hb][t - m], acc[t], 0, 0, 0);
          __builtin_amdgcn_sched_barrier(0);
        }
      }
    }
#pragma unroll
    for (int t = 0; t < 8; ++t) finish(wave, t, acc[t]);
  }
  STAMP(9);
  {
    const unsigned chb = 8 * A_CH;
    f32x4v acc0 = {0.f, 0.f, 0.f, 0.f}, acc1 = acc0;
    const int t = wave;
    bf16x8 H[2][4], X[2][4];
    auto rd4 = [&](int hb, int g4) {
#pragma unroll
      for (int k = 0; k < 4; ++k) { H[hb][k] = rdH(chb + 256 * g4, k); X[hb][k] = *(LAS bf16x8*)(lds + chb + aX + 512 * t - 256 * g4 - 64 * k); }
    };
    rd4(0, 0);
#pragma unroll 1
    for (int g8 = 0; g8 <= t; ++g8) {
      rd4(1, 2 * g8 + 1);
      __builtin_amdgcn_sched_barrier(0);
#pragma unroll
      for (int k = 0; k < 4; k += 2) { acc0 = __builtin_amdgcn_mfma_f32_16x16x32_bf16(H[0][k], X[0][k], acc0, 0, 0, 0); acc1 = __builtin_amdgcn_mfma_f32_16x16x32_bf16(H[0][k + 1], X[0][k + 1], acc1, 0, 0, 0); }
      __builtin_amdgcn_sched_barrier(0);
      rd4(0, g8 < t ? 2 * g8 + 2 : 0);
      __builtin_amdgcn_sched_barrier(0);
#pragma unroll
      for (int k = 0; k < 4; k += 2) { acc0 = __builtin_amdgcn_mfma_f32_16x16x32_bf16(H[1][k], X[1][k], acc0, 0, 0, 0); acc1 = __builtin_amdgcn_mfma_f32_16x16x32_bf16(H[1][k + 1], X[1][k + 1], acc1, 0, 0, 0); }
      __builtin_amdgcn_sched_barrier(0);
    }
    finish(8, t, acc0 + acc1);
  }
  publish_flag(p.ws, WS_FZ + a * FL_STRIDE, base + 1);
  STAMP(4);
  if (a == 63 && wave == 0) {
#pragma unroll 1
    for (int k = 0; k < 4; ++k) spin_flags(p.ws, rws, WS_ARR + (64 * k + lane) * FL_STRIDE, true, base, 0);
    if (lane == 0) flag_store(p.ws, WS_EPOCH, base >> 4);
  }
}

__global__ __launch_bounds__(512, 2) void k_main(Params p) {
  extern __shared__ __attribute__((aligned(16))) char smem[];
  LAS char* lds = (LAS char*)smem;
  const int b = blockIdx.x, g = b & 7, qq = b >> 3;
  u32x2 epv;
  asm volatile("global_load_dwordx2 %0, %1, %2 sc1" : "=v"(epv) : "v"(0u), "s"(p.ws) : "memory");
  if (qq < 24) mlp_role(p, lds, 8 * (g & 1) + qq / 3, g >> 1, qq % 3, epv);
  else aux_role(p, lds, (qq - 24) * 8 + g, qq - 24, g, epv);
}
}
#if defined(__HIP_DEVICE_COMPILE__)
#pragma clang attribute pop
#endif

extern "C" void kernel_launch(void* const* d_in, const int* in_sizes, int n_in, void* d_out, int out_size, void* d_ws, size_t ws_size, hipStream_t stream) {
  using namespace mk;
  Params p;
  p.x = (const u16*)d_in[0]; p.k = (const u16*)d_in[1]; p.v = (const u16*)d_in[2]; p.shk = (const u16*)d_in[3]; p.ssk = (const u16*)d_in[4]; p.ssd = (const u16*)d_in[5];
  p.w1 = (const u16*)d_in[7]; p.b1 = (const u16*)d_in[8]; p.w2 = (const u16*)d_in[9]; p.b2 = (const u16*)d_in[10];
  p.out = (u16*)d_out; p.ws = (char*)d_ws;
  (void)hipFuncSetAttribute((const void*)k_main, hipFuncAttributeMaxDynamicSharedMemorySize, LDS_BYTES);
  k_main<<<256, 512, LDS_BYTES, stream>>>(p);
}
```

```cpp
#include <hip/hip_runtime.h>
#include <stdint.h>
#include <type_traits>

#if defined(__HIP_DEVICE_COMPILE__)
#pragma clang attribute push(__attribute__((target("no-packed-fp32-ops"))), apply_to = function)
#endif
namespace mk {
constexpr int S_ = 2048, D_ = 576, F_ = 4608;
typedef unsigned short u16;
typedef unsigned long long u64;
typedef __bf16 bf16x8 __attribute__((ext_vector_type(8)));
typedef __bf16 bf16x2 __attribute__((ext_vector_type(2)));
typedef short s16x4 __attribute__((ext_vector_type(4)));
typedef short s16x8 __attribute__((ext_vector_type(8)));
typedef float f32x16 __attribute__((ext_vector_type(16)));
typedef float f32x4 __attribute__((ext_vector_type(4)));
typedef float f32x2 __attribute__((ext_vector_type(2)));
typedef unsigned u32x4 __attribute__((ext_vector_type(4)));
typedef unsigned u32x2 __attribute__((ext_vector_type(2)));
#define LAS __attribute__((address_space(3)))
#define GAS __attribute__((address_space(1)))
#define DI __device__ __forceinline__
typedef __amdgpu_buffer_rsrc_t rsrc_t;

constexpr unsigned FL_STRIDE = 128;
constexpr unsigned WS_EPOCH = 0;
constexpr unsigned WS_ARR   = 1024;
constexpr unsigned WS_PROG  = WS_ARR + 256 * FL_STRIDE;
constexpr unsigned WS_PFL   = WS_PROG + 192 * FL_STRIDE;
constexpr unsigned WS_FA    = WS_PFL + 768 * FL_STRIDE;
constexpr unsigned WS_FB    = WS_FA + 64 * FL_STRIDE;
constexpr unsigned WS_FZ    = WS_FB + 64 * FL_STRIDE;
constexpr unsigned WS_TMO   = WS_FZ + 64 * FL_STRIDE;
constexpr unsigned WS_FLAGS_END = 262144;
static_assert(WS_TMO + 128 <= WS_FLAGS_END, "flags");
constexpr unsigned WS_CS    = WS_FLAGS_END;
constexpr unsigned WS_KVT   = WS_CS + 8 * F_ * 4;
constexpr unsigned WS_ZT    = WS_KVT + D_ * S_ * 2;
constexpr unsigned WS_H     = WS_ZT + D_ * S_ * 2;
constexpr unsigned WS_PART  = WS_H + S_ * F_ * 2;
constexpr unsigned WS_STAMP = WS_PART + 48 * 16 * 12288;
constexpr unsigned WS_TL    = WS_STAMP + 256 * 64 * 8;
constexpr unsigned WS_END   = WS_TL + 256 * 1024 * 8;

struct Params {
  const u16 *x, *k, *v, *shk, *ssk, *ssd, *w1, *b1, *w2, *b2;
  u16* out; char* ws;
};

template <int I> using IC = std::integral_constant<int, I>;
template <int B, int E, class F> DI void sfor(F&& f) { if constexpr (B < E) { f(IC<B>{}); sfor<B + 1, E>(f); } }

DI float asf(unsigned u) { return __builtin_bit_cast(float, u); }
DI float b2f(u16 u) { return asf((unsigned)u << 16); }
DI float blo(unsigned u) { return asf(u << 16); }
DI float bhi(unsigned u) { return asf(u & 0xffff0000u); }
DI unsigned pk2(float a, float b) { f32x2 v = {a, b}; return __builtin_bit_cast(unsigned, __builtin_convertvector(v, bf16x2)); }
DI float rb(float f) { return blo(pk2(f, 0.f)); }
DI rsrc_t mkrsrc(const void* p, unsigned bytes) { return __builtin_amdgcn_make_buffer_rsrc((void*)p, 0, bytes, 0x00020000); }
DI unsigned rfl(unsigned v) { return __builtin_amdgcn_readfirstlane(v); }

DI void dma16(unsigned ldsaddr, unsigned voff, rsrc_t srd, unsigned soff) {
  asm volatile("s_mov_b32 m0, %0\n\ts_nop 0\n\tbuffer_load_dwordx4 %1, %2, %3 offen lds" :: "s"(ldsaddr), "v"(voff), "s"(srd), "s"(soff) : "memory");
}
template <unsigned MI, unsigned SI> DI void dma16i(unsigned mb, unsigned voff, rsrc_t srd, unsigned sb) {
  unsigned tmp;
  asm volatile("s_add_u32 m0, %1, %5\n\ts_add_u32 %0, %4, %6\n\tbuffer_load_dwordx4 %2, %3, %0 offen lds"
               : "=&s"(tmp) : "s"(mb), "v"(voff), "s"(srd), "s"(sb), "i"(MI), "i"(SI) : "scc", "memory");
}
template <unsigned SI> DI void ld16i_sc1(u32x4& dst, unsigned voff, rsrc_t srd, unsigned sb) {
  unsigned tmp;
  asm volatile("s_add_u32 %1, %4, %5\n\tbuffer_load_dwordx4 %0, %2, %3, %1 offen sc1" : "=v"(dst), "=&s"(tmp) : "v"(voff), "s"(srd), "s"(sb), "i"(SI) : "scc", "memory");
}
template <int WHICH> DI void dma16_if(unsigned sel, unsigned ldsaddr, unsigned voff, rsrc_t srd, unsigned soff) {
  asm volatile("s_cmp_lg_u32 %4, %5\n\ts_cbranch_scc1 .Ldma%=\n\ts_mov_b32 m0, %0\n\ts_nop 0\n\tbuffer_load_dwordx4 %1, %2, %3 offen lds\n.Ldma%=:"
               :: "s"(ldsaddr), "v"(voff), "s"(srd), "s"(soff), "s"(sel), "i"(WHICH) : "scc", "memory");
}
DI void ld16_sc1(u32x4& dst, unsigned voff, rsrc_t srd, unsigned soff) {
  asm volatile("buffer_load_dwordx4 %0, %1, %2, %3 offen sc1" : "=v"(dst) : "v"(voff), "s"(srd), "s"(soff) : "memory");
}
DI void ld16_sc1_s(u32x4& dst, unsigned voff, rsrc_t srd, unsigned soff) {
  asm volatile("s_nop 4\n\tbuffer_load_dwordx4 %0, %1, %2, %3 offen sc1" : "=v"(dst) : "v"(voff), "s"(srd), "s"(rfl(soff)) : "memory");
}
DI void ld8_sc1_s(u32x2& dst, unsigned voff, rsrc_t srd, unsigned soff) {
  asm volatile("s_nop 4\n\tbuffer_load_dwordx2 %0, %1, %2, %3 offen sc1" : "=v"(dst) : "v"(voff), "s"(srd), "s"(rfl(soff)) : "memory");
}
DI void ld8_sc1(u32x2& dst, unsigned voff, rsrc_t srd, unsigned soff) {
  asm volatile("buffer_load_dwordx2 %0, %1, %2, %3 offen sc1" : "=v"(dst) : "v"(voff), "s"(srd), "s"(soff) : "memory");
}
DI void ld4_sc1(unsigned& dst, unsigned voff, rsrc_t srd, unsigned soff) {
  asm volatile("buffer_load_dword %0, %1, %2, %3 offen sc1" : "=v"(dst) : "v"(voff), "s"(srd), "s"(soff) : "memory");
}
template <int N> DI void waitv() { asm volatile("s_waitcnt vmcnt(%0)" :: "i"(N) : "memory"); }
DI void bar() { __builtin_amdgcn_sched_barrier(0); asm volatile("s_barrier" ::: "memory"); __builtin_amdgcn_sched_barrier(0); }
DI void lgkm0() { asm volatile("s_waitcnt lgkmcnt(0)" ::: "memory"); }

DI int flane() { int l; asm volatile("v_mbcnt_lo_u32_b32 %0, -1, 0\n\tv_mbcnt_hi_u32_b32 %0, -1, %0" : "=v"(l)); return l; }
DI bool wall(bool c) { return __builtin_amdgcn_ballot_w64(!c) == 0; }
DI bool flag_ok(u32x2 v, u64 base, unsigned need) { u64 d = (((u64)v.y << 32) | v.x) - base; return d >= need && d <= 15; }
DI void flag_store(char* ws, unsigned off, u64 val) { __hip_atomic_store((GAS u64*)(ws + off), val, __ATOMIC_RELAXED, __HIP_MEMORY_SCOPE_AGENT); }
DI void spin_flags(char* ws, rsrc_t wsr, unsigned voff, bool active, u64 base, unsigned need) {
  const unsigned tag = (unsigned)base | 1u;
  for (unsigned it = 0;; ++it) {
    u32x2 v; ld8_sc1(v, voff, wsr, 0); asm volatile("s_waitcnt vmcnt(0)" : "+v"(v) :: "memory");
    if (wall(!active || flag_ok(v, base, need))) return;
    if (it > 30000u) { __hip_atomic_store((GAS unsigned*)(ws + WS_TMO), tag, __ATOMIC_RELAXED, __HIP_MEMORY_SCOPE_AGENT); return; }
    if ((it & 255u) == 255u && rfl(__hip_atomic_load((GAS unsigned*)(ws + WS_TMO), __ATOMIC_RELAXED, __HIP_MEMORY_SCOPE_AGENT)) == tag) return;
    __builtin_amdgcn_s_sleep(1);
  }
}
DI void sbar() { asm volatile("s_waitcnt lgkmcnt(0)\n\ts_barrier" ::: "memory"); }
DI void publish_flag(char* ws, unsigned off, u64 val) {
  asm volatile("s_waitcnt vmcnt(0) lgkmcnt(0)\n\ts_barrier" ::: "memory");
  if (threadIdx.x == 0) flag_store(ws, off, val);
}
DI u64 take_epoch(char* ws, const u32x2& epv) {
  unsigned lo, hi;
  asm volatile("v_readfirstlane_b32 %0, %2\n\tv_readfirstlane_b32 %1, %3\n\ts_nop 3" : "=s"(lo), "=s"(hi) : "v"(epv.x), "v"(epv.y) : "memory");
  const u64 base = ((((u64)hi << 32) | lo) + 1) << 4;
  if (threadIdx.x == 0) flag_store(ws, WS_ARR + blockIdx.x * FL_STRIDE, base);
  return base;
}
DI float wave_xor_add8(float v) {
  v += __builtin_bit_cast(float, __builtin_amdgcn_update_dpp(0, __builtin_bit_cast(int, v), 0xB1, 0xf, 0xf, true));
  v += __builtin_bit_cast(float, __builtin_amdgcn_update_dpp(0, __builtin_bit_cast(int, v), 0x4E, 0xf, 0xf, true));
  v += __builtin_bit_cast(float, __builtin_amdgcn_update_dpp(0, __builtin_bit_cast(int, v), 0x141, 0xf, 0xf, true));
  return v;
}

constexpr unsigned R1 = 0;
constexpr unsigned R2 = 9 * 8192;
constexpr unsigned L_MISC = R2 + 73728;
constexpr unsigned L_AUG = L_MISC;
constexpr unsigned L_B1RAW = L_AUG + 6144;
constexpr unsigned L_B2RAW = L_B1RAW + 1024;
constexpr unsigned L_HO = L_B2RAW + 1024;
constexpr unsigned L_ST = R2 + 65536;
constexpr unsigned L_Z = 24576;
constexpr unsigned L_XF = L_Z + 12288;
constexpr unsigned LDS_BYTES = 163840;

#define STAMP(n) do {} while (0)

#define TLK(T, K) do {} while (0)
#define TL(T) do {} while (0)
#define TLDUMP() do {} while (0)
namespace sch {
constexpr int NB = 18;
constexpr int kSel[NB]    = {    0,  0,  1,  2,  0,  1,  2,  0,  1,  0,  2,  0,  1,  2,  1,  2,  1,  2 };
constexpr int kUnit[NB]   = {    1,  0,  1,  1,  2,  0,  0,  3,  2,  4,  2,  5,  3,  3,  4,  4,  5,  5 };
constexpr int kStart[NB]  = {   12, 20, 23, 26, 29, 32, 35, 38, 41, 44, 47, 50, 53, 56, 59, 62, 65, 68 };
constexpr int kRing[NB]   = {   -1, -1,  0,  1, -1,  2,  0, -1,  1, -1,  2, -1,  0,  1,  2,  0,  1,  2 };
constexpr int kHIssue[NB] = {   -1, -1, 20, 21, -1, 27, 29, -1, 36, -1, 41, -1, 47, 50, 54, 56, 59, 62 };
constexpr int kW2Buf[NB]  = {    3,  4,  5,  3,  4,  5,  3,  4,  5,  3,  4,  0,  1,  2,  5,  3,  4,  0 };
constexpr int bsel(int b) { return kSel[b]; }
constexpr int bunit(int b) { return kUnit[b]; }
constexpr int bstart(int b) { return kStart[b]; }
constexpr int NST = bstart(NB - 1) + 3;
constexpr int g_ks(int b, int G) { return b == NB - 1 ? G % 4 : G / 6; }
constexpr int g_tn(int b, int G) { return b == NB - 1 ? G / 4 : G % 6; }
constexpr int blk_of(int T) { for (int b = 0; b < NB; ++b) if (T >= bstart(b) && T < bstart(b) + 3) return b; return -1; }
constexpr int gelu0(int u) { return u == 0 ? 9 : u == 5 ? 45 : 9 * u; }
constexpr int chunkW(int u, int k) { return u == 5 ? 46 + k : gelu0(u) + 2 + 2 * k; }
constexpr int chunkR(int u, int k) { return (u == 0 && k == 0) ? 13 : chunkW(u, k) + 1; }
constexpr bool ho_written(int T) { for (int u = 0; u < 6; ++u) if (u != 1) for (int k = 0; k < 4; ++k) if (chunkW(u, k) == T) return true; return false; }
constexpr int cr_unit(int T) { for (int u = 0; u < 6; ++u) if (u != 1) for (int k = 0; k < 4; ++k) if (chunkR(u, k) == T) return u; return -1; }
constexpr int cr_k(int T) { for (int u = 0; u < 6; ++u) if (u != 1) for (int k = 0; k < 4; ++k) if (chunkR(u, k) == T) return k; return -1; }
constexpr int pubK(int T) { return T == 16 ? 1 : T == 23 ? 2 : T == 32 ? 3 : T == 41 ? 4 : T == 50 ? 5 : T == 55 ? 6 : 0; }
constexpr int c1_unit(int c) { return c < 18 ? (c & 1) : 2 + (c - 18) / 9; }
constexpr int c1_kc(int c) { return c < 18 ? (c >> 1) : (c - 18) % 9; }
#define W1LEAD 6
constexpr int c1_issue(int c) { return c < 9 ? -1 : c < 25 ? (c - 9) / 2 + 1 : c - 9 - W1LEAD < 11 ? 11 : c - 9 - W1LEAD; }
constexpr int c1_use(int c) { return c < 18 ? (c >> 1) : c - 9; }
constexpr int x_issue(int kc) { return kc < 4 ? -1 : kc - 3; }
constexpr int x_pos(int kc) { return (kc + 3) & 3; }
constexpr int w2_issue(int b) { return b == 0 ? 7 : b <= 10 ? bstart(b) - 6 : bstart(b) - 9; }
constexpr int w2_buf(int b) { return kW2Buf[b]; }
constexpr int h_issue(int b) { return kHIssue[b]; }
constexpr bool w1_by_C(int T) { return !((T >= 12 && T <= 14) || T >= 20); }
#define W2P 1
constexpr bool w2_by_C(int T) { return W2P ? (T < 12 || (T >= 15 && T < 20)) : T < 45; }
constexpr int nD(int T, bool C) {
  int n = 0;
  for (int c = 9; c < 54; ++c) if (c1_issue(c) == T) n += T < 9 ? 1 : (w1_by_C(T) == C) ? 2 : 0;
  for (int kc = 4; kc < 9; ++kc) if (x_issue(kc) == T) n += 2;
  for (int b = 0; b < NB; ++b) if (T >= w2_issue(b) && T < w2_issue(b) + 3) n += b == 0 ? 1 : (w2_by_C(T) == C) ? 2 : 0;
  return n;
}
#define HSPREAD 1
constexpr int nH(int T) { int n = 0; for (int b = 2; b < NB; ++b) { if (h_issue(b) < 0) continue; if (HSPREAD) { if (h_issue(b) == T || h_issue(b) + 1 == T) n += 2; } else if (h_issue(b) == T) n += 4; } return n; }
constexpr int cr_unit(int T);
constexpr int nS(int T) { return (T >= 12 && cr_unit(T) >= 0) ? 1 : 0; }
constexpr int cnt(int T, bool C) { return nD(T, C) + (C ? nH(T) + nS(T) : 0); }
constexpr int after(int t0, int T, bool C) { int n = 0; for (int t = t0 + 1; t < T; ++t) n += cnt(t, C); return n; }
constexpr int req(int T, bool C) {
  int r = -2;
  auto mx = [&r](int v) { r = r > v ? r : v; };
  for (int tt = T; tt <= T + 1; ++tt) {
    if (tt == T + 1 && T < 9) break;
    if (tt < 9) { mx(c1_issue(2 * tt)); mx(c1_issue(2 * tt + 1)); mx(x_issue(tt)); }
    else {
      if (tt < 45) { const int t = c1_issue(tt + 9); if (t < 9 || w1_by_C(t) == C) mx(t); }
      const int b = blk_of(tt);
      if (b >= 0) for (int k = 0; k < 3; ++k) { const int t = w2_issue(b) + k; if (b == 0 || w2_by_C(t) == C) mx(t); }
    }
  }
  if (C) { const int b = blk_of(T); if (b >= 2 && bstart(b) == T && h_issue(b) >= 0) mx(h_issue(b) + (HSPREAD ? 1 : 0)); }
  return r;
}
static_assert(cnt(0, true) == 0 && cnt(0, false) == 0 && cnt(1, true) == 4 && cnt(1, false) == 4, "nothing is issued in stage 0");
constexpr int c1_first(int T) { for (int c = 9; c < 54; ++c) if (c1_issue(c) == T) return c; return -1; }
constexpr int c1_second(int T) { for (int c = 10; c < 54; ++c) if (c1_issue(c) == T && c1_issue(c - 1) == T) return c; return -1; }
constexpr int x_at(int T) { for (int kc = 4; kc < 9; ++kc) if (x_issue(kc) == T) return kc; return -1; }
constexpr int w2_at(int T, int k = 0) { for (int b = 0; b < NB; ++b) if (T >= w2_issue(b) && T < w2_issue(b) + 3) { if (k == 0) return b; --k; } return -1; }
constexpr int h_at(int T) { for (int b = 2; b < NB; ++b) if (h_issue(b) == T) return b; return -1; }
constexpr int chk_at(int T) { const int b = h_at(T); return (b >= 2 && bsel(b) == 1) ? b : -1; }
constexpr int val_at(int G, int G0, int RN, int RD, int k) { for (int v = 0; v < 32; ++v) if (G0 + v * RN / RD == G) { if (k == 0) return v; --k; } return -1; }
constexpr int PRO = 9 + 8;
constexpr int waitn(int T, bool C) {
  const int r = req(T, C);
  if (r == -2) return 63;
  if (r >= 0) return after(r, T, C);
  const int need = T < 4 ? 4 * (T + 1) : PRO;
  return PRO - need + after(-1, T, C);
}
}

DI void mlp_role(const Params& p, LAS char* lds, const int i, const int s, const int jn, u32x2 epv) {
  u64 base = 0;
  using namespace sch;
  const int tid = threadIdx.x, lane = tid & 63;
  const int wave = rfl(tid >> 6), rg = wave & 3, role = wave >> 2;
  const int j = 3 * s + jn;
  const int m = lane & 31, hh = lane >> 5, blk = (lane >> 4) & 1, q = (lane & 15) >> 2, pp = lane & 3;
  const rsrc_t rx = mkrsrc(p.x, S_ * D_ * 2), rw1 = mkrsrc(p.w1, D_ * F_ * 2), rw2 = mkrsrc(p.w2, F_ * D_ * 2);
  const rsrc_t rws = mkrsrc(p.ws, WS_END);
  const unsigned ldsb = (unsigned)(size_t)lds;
  STAMP(0);
  const unsigned voX = (8 * wave + (lane >> 3)) * 1152 + (((lane & 7) ^ ((4 * (wave & 1) + (lane >> 4)) & 7)) * 16);
  const unsigned voW1 = (8 * wave + (lane >> 3)) * 9216 + (((lane & 7) ^ (((lane >> 4) & 1) << 2)) * 16);
  unsigned voW2[3];
#pragma unroll
  for (int ii = 0; ii < 3; ++ii) { const unsigned L = 1024 * (3 * wave + ii) + 16 * lane, row = L / 384, ch = (L % 384) / 16; voW2[ii] = row * 1152 + ((ch ^ ((row >> 1) & 1)) * 16); }
  const int jA = 3 * s + (jn + 1) % 3, jB = 3 * s + (jn + 2) % 3;
  const unsigned mW = ldsb + wave * 1024, mW3 = ldsb + wave * 3072, mC2 = ldsb + rg * 2048, mC6 = ldsb + rg * 6144;
  const unsigned sW1 = 768 * j, sX = 128 * i * 1152;
  const unsigned sW2[3] = {(unsigned)(384 * j * 1152 + 384 * jn), (unsigned)(384 * jA * 1152 + 384 * jn), (unsigned)(384 * jB * 1152 + 384 * jn)};
  auto dmaW1 = [&](auto Cc) { constexpr int c = decltype(Cc)::value;
    dma16i<R1 + (c % 9) * 8192, (64 * c1_kc(c)) * 9216 + 128 * c1_unit(c)>(mW, voW1, rw1, sW1); };
  auto dmaX = [&](auto Kc) { constexpr int kc = decltype(Kc)::value;
    dma16i<R2 + x_pos(kc) * 16384, kc * 128>(mW, voX, rx, sX);
    dma16i<R2 + x_pos(kc) * 16384 + 8192, 64 * 1152 + kc * 128>(mW, voX, rx, sX); };
  auto dmaW2 = [&](auto Bc, auto Kc) { constexpr int b = decltype(Bc)::value, k = decltype(Kc)::value;
    dma16i<w2_buf(b) * 24576 + k * 1024, 64 * bunit(b) * 1152>(mW3, voW2[k], rw2, sW2[bsel(b)]); };
  const unsigned voW1c = (16 * rg + (lane >> 3)) * 9216 + (((lane & 7) ^ (((lane >> 4) & 1) << 2)) * 16);
  unsigned voW2c[3];
  auto mkW2c = [&](int l) {
#pragma unroll
    for (int ii = 0; ii < 3; ++ii) { const unsigned L = 1024 * ii + 16 * l, row = L / 384, ch = (L % 384) / 16; voW2c[ii] = (16 * rg + row) * 1152 + ((ch ^ ((row >> 1) & 1)) * 16); }
  };
  auto dmaW1c = [&](auto Cc, auto Hc) { constexpr int c = decltype(Cc)::value, h = decltype(Hc)::value;
    dma16i<R1 + (c % 9) * 8192 + h * 1024, (64 * c1_kc(c) + 8 * h) * 9216 + 128 * c1_unit(c)>(mC2, voW1c, rw1, sW1); };
  auto dmaW2c = [&](auto Bc, auto Kc) { constexpr int b = decltype(Bc)::value, k = decltype(Kc)::value;
    dma16i<w2_buf(b) * 24576 + k * 1024, (64 * bunit(b) + 8 * (k / 3)) * 1152>(mC6, voW2c[k % 3], rw2, sW2[bsel(b)]); };
  auto site = [&](auto Tc, auto Sc, auto Cr) {
    constexpr int T = decltype(Tc)::value, st = decltype(Sc)::value; constexpr bool C = decltype(Cr)::value;
    if constexpr (T < 9) {
      if constexpr (st == 0 && c1_first(T) >= 0) dmaW1(IC<c1_first(T)>{});
      if constexpr (st == 1 && c1_second(T) >= 0) dmaW1(IC<c1_second(T)>{});
      if constexpr (st == 2 && x_at(T) >= 0) dmaX(IC<x_at(T)>{});
    } else if constexpr (w1_by_C(T) == C) {
      if constexpr (st < 2 && c1_first(T) >= 0) dmaW1c(IC<c1_first(T)>{}, Sc);
      if constexpr (st < 2 && c1_second(T) >= 0) dmaW1c(IC<c1_second(T)>{}, Sc);
    }
    if constexpr (w2_at(T) == 0) { if constexpr (st == 2) dmaW2(IC<0>{}, IC<T - w2_issue(0)>{}); }
    else if constexpr (w2_by_C(T) == C && w2_at(T) > 0 && st >= 2) dmaW2c(IC<w2_at(T)>{}, IC<2 * (T - w2_issue(w2_at(T) < 0 ? 0 : w2_at(T))) + (st - 2)>{});
    if constexpr (w2_by_C(T) == C && w2_at(T, 1) > 0 && st < 2) dmaW2c(IC<w2_at(T, 1)>{}, IC<2 * (T - w2_issue(w2_at(T, 1) < 0 ? 0 : w2_at(T, 1))) + st>{});
  };
  auto allsites = [&](auto Tc, auto Cr) { site(Tc, IC<0>{}, Cr); site(Tc, IC<1>{}, Cr); site(Tc, IC<2>{}, Cr); site(Tc, IC<3>{}, Cr); };
  if (wave == 0) dma16(ldsb + L_B1RAW, lane * 16, mkrsrc(p.b1 + 384 * j, 768), 0);
  if (wave == 1) dma16(ldsb + L_B2RAW, lane * 16, mkrsrc(p.b2 + 192 * jn, 384), 0);
  sfor<0, 4>([&](auto Kc) { constexpr int k = decltype(Kc)::value; dmaX(Kc); dmaW1(IC<2 * k>{}); dmaW1(IC<2 * k + 1>{}); });
  dmaW1(IC<8>{});

  const int sw = (q >> 1) & 1;
  const unsigned a1row = ldsb + R1 + (8 * hh + q) * 128 + 32 * blk + 8 * pp;
  const unsigned a1e = a1row + 64 * sw, a1o = a1row + 64 * (1 - sw);
  const unsigned a2row = ldsb + (4 * hh + q) * 384 + 96 * (2 * blk + (pp >> 1)) + 8 * (pp & 1);
  const unsigned a2e = a2row + 16 * sw, a2o = a2row + 16 * (1 - sw);
  const unsigned ax = ldsb + R2 + (32 * rg + m) * 128 + ((hh ^ ((m >> 1) & 7)) * 16);
  auto trr = [&](unsigned addr, unsigned d) -> bf16x8 {
    s16x4 lo = __builtin_amdgcn_ds_read_tr16_b64_v4i16((LAS s16x4*)(size_t)addr);
    s16x4 hi = __builtin_amdgcn_ds_read_tr16_b64_v4i16((LAS s16x4*)(size_t)(addr + d));
    return __builtin_bit_cast(bf16x8, __builtin_shufflevector(lo, hi, 0, 1, 2, 3, 4, 5, 6, 7));
  };
  const f32x16 zero16 = {};
  const unsigned hbase_own = WS_H + (unsigned)(i * 12 + j) * 98304u;
  const unsigned voH = rg * 4096 + lane * 16;
  float sd = 1.f, k0 = 0.f, k1 = 0.f, mean_f = 0.f;
  bf16x8 augB = {};
  u32x2 pollv = {0u, 0u};
  float st2[4] = {0.f, 0.f, 0.f, 0.f};
  f32x4 st1 = {0.f, 0.f, 0.f, 0.f};
  unsigned csv[8];
  auto actFA = [&](int l) { return l < 16 && (384 * j) / 576 + (l >> 3) <= (384 * j + 383) / 576; };
  auto voFA = [&](int l) -> unsigned { return actFA(l) ? WS_FA + (((384 * j) / 576 + (l >> 3)) * 8 + (l & 7)) * FL_STRIDE : WS_FA; };

  bf16x8 xr[36];
  f32x16 acc[2][2];
#define PFP_ 4
  constexpr int PF = 4, PFP = PFP_;
  bf16x8 pf[PF];
  unsigned b1e = a1e + role * 8192, b1o = a1o + role * 8192, b1en = 0, b1on = 0;
  asm volatile("" : "+v"(b1e), "+v"(b1o));
  auto rd1 = [&](auto Ic) -> bf16x8 { constexpr int I = decltype(Ic)::value; return trr(((I & 1) ? b1o : b1e) + (I >> 1) * 2048, 512); };
  auto rd1n = [&](auto Dc, auto Ic) -> bf16x8 { constexpr int D = decltype(Dc)::value, I = decltype(Ic)::value;
    if constexpr (D > 0) return trr(((I & 1) ? b1o : b1e) + D * 8192 + (I >> 1) * 2048, 512);
    else return trr(((I & 1) ? b1on : b1en) + (I >> 1) * 2048, 512); };
  auto adv1 = [&](auto Dc) { constexpr int D = decltype(Dc)::value; b1e += D * 8192; b1o += D * 8192; asm volatile("" : "+v"(b1e), "+v"(b1o)); };
  u32x4 hv[4];
  const unsigned aHO = ldsb + L_HO + rg * 1024 + lane * 16;
  float ez[32], eo[32];
  auto epigap = [&](auto Uc, auto Bc, auto Gc, auto G0c, auto RNc, auto RDc) {
    constexpr int uu = decltype(Uc)::value, BUF = decltype(Bc)::value, G = decltype(Gc)::value, G0 = decltype(G0c)::value, RN = decltype(RNc)::value, RD = decltype(RDc)::value;
    sfor<0, 2>([&](auto Kc) { constexpr int v = val_at(G - 3, G0, RN, RD, decltype(Kc)::value); if constexpr (v >= 0) ez[v] = __builtin_fmaf(ez[v], sd, sd); });
    sfor<0, 2>([&](auto Kc) { constexpr int v = val_at(G, G0, RN, RD, decltype(Kc)::value);
      if constexpr (v >= 0) { const float w = acc[BUF][v >> 4][v & 15], w2 = w * w, pz = __builtin_fmaf(k1, w2, k0); ez[v] = pz * w; } });
    sfor<0, 2>([&](auto Kc) { constexpr int v = val_at(G - 5, G0, RN, RD, decltype(Kc)::value);
      if constexpr (v >= 0) eo[v] = acc[BUF][v >> 4][v & 15] * ez[v]; });
    sfor<0, 2>([&](auto Kc) { constexpr int v = val_at(G - 3, G0, RN, RD, decltype(Kc)::value); if constexpr (v >= 0) ez[v] = __builtin_amdgcn_rcpf(ez[v]); });
    sfor<0, 2>([&](auto Kc) { constexpr int v = val_at(G - 2, G0, RN, RD, decltype(Kc)::value); if constexpr (v >= 0) ez[v] = __builtin_amdgcn_exp2f(ez[v]); });
    sfor<0, 2>([&](auto Kc) { constexpr int v = val_at(G - 5, G0, RN, RD, decltype(Kc)::value);
      if constexpr (v >= 0) {
        if constexpr (v & 1) hv[v >> 3][(v & 7) >> 1] = pk2(eo[v - 1], eo[v]);
        if constexpr ((v & 7) == 7) {
          if constexpr (uu == 1) {
            __builtin_amdgcn_raw_buffer_store_b128(hv[v >> 3], rws, voH + (v >> 3) * 1024, hbase_own + uu * 16384, 16);
          } else *(LAS u32x4*)(size_t)(aHO + ((v >> 3) & 1) * 4096) = hv[v >> 3];
        }
      } });
  };
  auto aug = [&](auto Uc, auto Bc) {
    constexpr int uu = decltype(Uc)::value, BUF = decltype(Bc)::value;
#pragma unroll
    for (int t = 0; t < 2; ++t) {
      const bf16x8 af = *(LAS bf16x8*)(lds + L_AUG + (64 * uu + 32 * t + m) * 16);
      acc[BUF][t] = __builtin_amdgcn_mfma_f32_32x32x16_bf16(af, augB, acc[BUF][t], 0, 0, 0);
    }
  };
  auto g1stage = [&](auto Tc, auto Cc, auto Bc, auto Nc, auto Cr, auto&& work, auto&& syncnext) {
    constexpr int T = decltype(Tc)::value, c = decltype(Cc)::value, BUF = decltype(Bc)::value, NEXT = decltype(Nc)::value, kc = c1_kc(c);
    constexpr int PF = T < 9 ? 4 : PFP, G0 = T < 9 ? 0 : (8 * (T - 9)) % PF;
    constexpr int DN = NEXT >= 0 ? NEXT % 9 - c % 9 : 1;
    if constexpr (NEXT >= 0 && DN < 0) { b1en = b1e + DN * 8192; b1on = b1o + DN * 8192; asm volatile("" : "+v"(b1en), "+v"(b1on)); }
    __builtin_amdgcn_sched_barrier(0);
    sfor<0, 8>([&](auto Ic) {
      constexpr int I = decltype(Ic)::value;
      const bf16x8 cur = pf[(G0 + I) % PF];
      if constexpr (I + PF < 8) pf[(G0 + I) % PF] = rd1(IC<I + PF>{});
      else if constexpr (NEXT >= 0) pf[(G0 + I) % PF] = rd1n(IC<DN>{}, IC<I + PF - 8>{});
      acc[BUF][I & 1] = __builtin_amdgcn_mfma_f32_32x32x16_bf16(cur, xr[4 * kc + (I >> 1)], (kc == 0 && I < 2) ? zero16 : acc[BUF][I & 1], 0, 0, 0);
      __builtin_amdgcn_sched_barrier(0);
      if constexpr (I == 7 && NEXT >= 0) adv1(IC<DN>{});
      if constexpr (T < 10) {
        if constexpr (I == 1) site(Tc, IC<0>{}, Cr);
        if constexpr (I == 3) site(Tc, IC<1>{}, Cr);
        if constexpr (I == 5) site(Tc, IC<2>{}, Cr);
      } else {
        if constexpr (I == 0) site(Tc, IC<0>{}, Cr);
        if constexpr (I == 2) site(Tc, IC<1>{}, Cr);
        if constexpr (I == 4) site(Tc, IC<2>{}, Cr);
        if constexpr (I == 6) site(Tc, IC<3>{}, Cr);
      }
      if constexpr (I == 7) { asm volatile("" : "+v"(acc[BUF][0]), "+v"(acc[BUF][1])); syncnext(); }
      work(Ic, cur);
      __builtin_amdgcn_sched_barrier(0);
    });
  };
  auto dot2 = [](unsigned a, unsigned b, float c) { return __builtin_amdgcn_fdot2_f32_bf16(__builtin_bit_cast(bf16x2, a), __builtin_bit_cast(bf16x2, b), c, false); };
  const unsigned selw = ((lane & 15) == ((lane >> 4) & 1)) ? 0x3f803f80u : 0u;
  const bf16x8 selA = __builtin_bit_cast(bf16x8, u32x4{selw, selw, selw, selw});
  auto xfinish = [&]() {
    auto both = [](float v) { const unsigned u = __builtin_bit_cast(unsigned, v); const auto r = __builtin_amdgcn_permlane32_swap(u, u, false, false); return asf(r[0]) + asf(r[1]); };
    const float t2 = both((st2[0] + st2[1]) + (st2[2] + st2[3]));
    const int l = flane();
    if (l < 16) { *(LAS float*)(lds + L_ST + role * 512 + (32 * rg + l) * 4) = st1[0]; *(LAS float*)(lds + L_ST + role * 512 + (32 * rg + 16 + l) * 4) = st1[1]; }
    if (l < 32) *(LAS float*)(lds + L_ST + 1024 + role * 512 + (32 * rg + l) * 4) = t2;
    asm volatile("" : "+v"(csv[0]), "+v"(csv[1]), "+v"(csv[2]), "+v"(csv[3]), "+v"(csv[4]), "+v"(csv[5]), "+v"(csv[6]), "+v"(csv[7]));
    if (wave < 6) {
      const float cs = ((asf(csv[0]) + asf(csv[1])) + (asf(csv[2]) + asf(csv[3]))) + ((asf(csv[4]) + asf(csv[5])) + (asf(csv[6]) + asf(csv[7]))), ch = rb(cs);
      const int f = 64 * wave + l;
      *(LAS u32x4*)(lds + L_AUG + f * 16) = u32x4{pk2(ch, cs - ch), *(LAS u16*)(lds + L_B1RAW + f * 2), 0u, 0u};
    }
    lgkm0();
  };
  auto takestats = [&]() {
    const LAS float* q = (const LAS float*)(lds + L_ST) + 32 * rg + m;
    const float s1 = q[0] + q[128], s2 = q[256] + q[384];
    const float mean = s1 * (1.0f / D_), var = __builtin_fmaxf(__builtin_fmaf(-mean, mean, s2 * (1.0f / D_)), 0.f);
    mean_f = rb(mean); sd = rb(__builtin_sqrtf(rb(rb(var) + 1e-5f)));
    const float r = 1.0f / sd; k0 = -2.302208198f * r; k1 = -0.1029432397f * r * r * r;
    const u32x4 bb = {hh ? 0u : (pk2(-mean_f, -mean_f)), hh ? 0u : (pk2(sd, 0.f)), 0u, 0u};
    augB = __builtin_bit_cast(bf16x8, bb);
  };
  auto syncX = [&](auto Tc, auto Cr) {
    constexpr int T = decltype(Tc)::value; constexpr bool C = decltype(Cr)::value;
    if constexpr (T == 8) {
      if (wave == 0) { waitv<after(5, 8, C)>(); asm volatile("" : "+v"(pollv)); const int l = flane(); if (!wall(!actFA(l) || flag_ok(pollv, base, 1))) spin_flags(p.ws, rws, voFA(l), actFA(l), base, 1); }
    }
    waitv<waitn(T, C)>();
    bar();
    if constexpr (T == 0) { base = take_epoch(p.ws, epv); STAMP(1); }
    if constexpr (T == 6) { if (wave == 0) ld8_sc1(pollv, voFA(flane()), rws, 0); }
    if constexpr (T == 8) {
      const unsigned vo = (384 * j + 64 * (wave < 6 ? wave : 0) + flane()) * 4;
#pragma unroll
      for (int r = 0; r < 8; ++r) ld4_sc1(csv[r], vo, rws, WS_CS + r * F_ * 4);
    }
  };
  auto xphase = [&](auto Cr, auto&& sync9) {
    constexpr bool C = decltype(Cr)::value;
    syncX(IC<0>{}, Cr);
    sfor<0, 9>([&](auto Tc) {
      constexpr int T = decltype(Tc)::value;
      constexpr int cc = 2 * T + (C ? 1 : 0);
      if constexpr (T > 0) adv1(IC<cc % 9 - (cc - 2) % 9>{});
      sfor<0, 4>([&](auto Kc) { constexpr int ks = decltype(Kc)::value;
        xr[4 * T + ks] = *(LAS bf16x8*)(size_t)((ax ^ (ks << 5)) + x_pos(T) * 16384); });
      sfor<0, PF>([&](auto Ic) { pf[decltype(Ic)::value] = rd1(Ic); });
      g1stage(Tc, IC<2 * T + (C ? 1 : 0)>{}, IC<(C ? 1 : 0)>{}, IC<-1>{}, Cr, [&](auto Ic, const bf16x8& cur) {
        constexpr int I = decltype(Ic)::value;
        if constexpr ((I & 1) == 0 && ((I >> 2) == (C ? 1 : 0))) {
          st1 = __builtin_amdgcn_mfma_f32_16x16x32_bf16(selA, xr[4 * T + (I >> 1)], st1, 0, 0, 0);
          const u32x4 x = __builtin_bit_cast(u32x4, xr[4 * T + (I >> 1)]);
#pragma unroll
          for (int e = 0; e < 4; ++e) st2[e] = dot2(x[e], x[e], st2[e]);
        }
      }, [&]() { if constexpr (T < 8) syncX(IC<T + 1>{}, Cr); else sync9(); });
    });
  };
  if (role == 0) {
    u32x4 zq[3];
    auto syncP = [&](auto Tc) {
      constexpr int T = decltype(Tc)::value;
      if constexpr (T == 9) { waitv<cnt(8, false)>(); xfinish(); }
      waitv<(T == 67 ? 0 : waitn(T, false))>();
      if constexpr (T > 45 && T <= 50) lgkm0();
      if constexpr (T - 1 < 45 && ho_written(T - 1)) asm volatile("s_waitcnt lgkmcnt(6)" ::: "memory");
      bar();
      if constexpr (T == 9) takestats();
    };
    xphase(std::false_type{}, [&]() { syncP(IC<9>{}); });
    if constexpr (W2P) mkW2c(flane());
    STAMP(2);
    sfor<9, NST>([&](auto Tc) {
      constexpr int T = decltype(Tc)::value;
      if constexpr (T < 45) {
        constexpr int u = 2 + (T - 9) / 9, kc = (T - 9) % 9, PU = u == 2 ? 0 : u - 1;
        constexpr int UB = (u + 1) & 1, EB = UB ^ 1;
        if constexpr (T == 9) { adv1(IC<18 % 9 - 16 % 9>{}); sfor<0, PFP>([&](auto Ic) { pf[decltype(Ic)::value] = rd1(Ic); }); }
        if constexpr (kc == 0) aug(IC<PU>{}, IC<EB>{});
        g1stage(Tc, IC<T + 9>{}, IC<UB>{}, IC<(T + 1 < 45 ? T + 10 : -1)>{}, std::false_type{}, [&](auto Ic, const bf16x8&) {
          epigap(IC<PU>{}, IC<EB>{}, IC<8 * kc + decltype(Ic)::value>{}, IC<1>{}, IC<2>{}, IC<1>{}); }, [&]() { syncP(IC<T + 1>{}); });
      } else {
        if constexpr (T == 45) { STAMP(3); aug(IC<5>{}, IC<0>{}); if constexpr (!W2P) mkW2c(flane()); }
        allsites(Tc, std::false_type{});
        if constexpr (T == 60) {
          const int l = flane();
#pragma unroll
          for (int ii = 0; ii < 3; ++ii) { const int pc = 3 * rg + ii;
            ld16_sc1_s(zq[ii], (4 * pc + (l >> 4)) * 4096 + (l & 15) * 16, rws, WS_ZT + (192 * jn + 48 * s) * 4096 + (128 * i) * 2); }
#pragma unroll
          for (int ii = 0; ii < 3; ++ii) { const unsigned L = (3 * rg + ii) * 1024 + l * 16, row = L / 96, col = L - 96 * row;
            dma16(rfl(ldsb + L_XF + (3 * rg + ii) * 1024), row * 1152 + col, rx, rfl((128 * i) * 1152 + (192 * jn + 48 * s) * 2)); }
        }
        if constexpr (T == 67) {
          asm volatile("" : "+v"(zq[0]), "+v"(zq[1]), "+v"(zq[2]));
          const int l = flane();
#pragma unroll
          for (int ii = 0; ii < 3; ++ii) *(LAS u32x4*)(lds + L_Z + (3 * rg + ii) * 1024 + l * 16) = zq[ii];
          lgkm0();
        }
        if constexpr (T < 50) sfor<0, 8>([&](auto Ic) { epigap(IC<5>{}, IC<0>{}, IC<8 * (T - 45) + decltype(Ic)::value>{}, IC<1>{}, IC<1>{}, IC<1>{}); });
        if constexpr (T + 1 < NST) syncP(IC<T + 1>{});
      }
    });
    return;
  }
  f32x16 acc2[6];
  u32x4 hring[3][4], hown[4];
  unsigned b2e[2], b2o[2];
  auto base2 = [&](auto Bc) { constexpr int b = decltype(Bc)::value;
    unsigned e = a2e + w2_buf(b) * 24576, o = a2o + w2_buf(b) * 24576; asm volatile("" : "+v"(e), "+v"(o)); b2e[b & 1] = e; b2o[b & 1] = o; };
  auto rd2 = [&](auto Bc, auto Gc) -> bf16x8 { constexpr int b = decltype(Bc)::value, G = decltype(Gc)::value, ks = g_ks(b, G), tn = g_tn(b, G);
    return trr(((tn & 1) ? b2o[b & 1] : b2e[b & 1]) + ks * 16 * 384 + 32 * (tn >> 1), 8 * 384); };
  const unsigned hbA = WS_H + (unsigned)(i * 12 + jA) * 98304u, hbB = WS_H + (unsigned)(i * 12 + jB) * 98304u;
  const unsigned sH[3] = {hbase_own, hbA, hbB};
  auto issueH = [&](auto Tc, auto Gc) {
    constexpr int T = decltype(Tc)::value, G = decltype(Gc)::value;
    sfor<0, 2>([&](auto Hc) {
      constexpr int half = decltype(Hc)::value, b = h_at(HSPREAD ? T - half : T);
      if constexpr (b >= 2) {
        constexpr int r = kRing[b], k = 2 * half + G;
        ld16i_sc1<bunit(b) * 16384 + k * 1024>(hring[r][k], voH, rws, sH[bsel(b)]);
      }
    });
  };
  u32x2 progv = {0u, 0u};
  unsigned proga = WS_PROG + (i * 12 + j) * FL_STRIDE;
  auto voPR = [&](int l) -> unsigned { return WS_PROG + (i * 12 + (l == 0 ? jA : jB)) * FL_STRIDE; };
  auto actFZ = [&](int l) { return ((192 * jn + 48 * s) / 9 + l) <= (192 * jn + 48 * s + 47) / 9; };
  auto voFZ = [&](int l) -> unsigned { return actFZ(l) ? WS_FZ + ((192 * jn + 48 * s) / 9 + l) * FL_STRIDE : WS_FZ; };
  u32x2 pollz = {0u, 0u};
  constexpr auto needOf = [](int b) { return sch::bunit(b) == 1 ? 1 : sch::bunit(b) == 0 ? 2 : sch::bunit(b) + 1; };
  auto syncC = [&](auto Tc) {
    constexpr int T = decltype(Tc)::value, b = blk_of(T);
    if constexpr (T == 9) { waitv<cnt(8, true)>(); xfinish(); }
    if constexpr (chk_at(T) >= 0) {
      constexpr int bb = chk_at(T);
      if (wave == 4) { waitv<after(T - 3, T, true)>(); asm volatile("" : "+v"(pollv)); const int l = flane();
        if (!wall(l >= 2 || flag_ok(pollv, base, needOf(bb)))) spin_flags(p.ws, rws, voPR(l), l < 2, base, needOf(bb));
        if constexpr (bb == NB - 2) { asm volatile("" : "+v"(pollz)); if (!wall(!actFZ(l) || flag_ok(pollz, base, 1))) spin_flags(p.ws, rws, voFZ(l), actFZ(l), base, 1); } }
    }
    if constexpr (pubK(T) > 0) waitv<after(T - 5, T, true)>();
    waitv<waitn(T, true)>();
    if constexpr (b >= 2 && bstart(b < 0 ? 0 : b) == T && kRing[b < 0 ? 0 : b] >= 0) { constexpr int r = kRing[b < 0 ? 0 : b]; asm volatile("" : "+v"(hring[r][0]), "+v"(hring[r][1]), "+v"(hring[r][2]), "+v"(hring[r][3])); }
    bar();
    if constexpr (pubK(T) > 0) {
      progv.x += 1;
      if (wave == 4) asm volatile("s_mov_b64 exec, 1\n\tbuffer_store_dwordx2 %0, %1, %2, 0 offen sc1\n\ts_mov_b64 exec, -1" :: "v"(progv), "v"(proga), "s"(rws) : "memory");
    }
    if constexpr (T == 9) takestats();
    if constexpr (chk_at(T + 2) >= 0) {
      if (wave == 4) { const int l = flane(); ld8_sc1(pollv, voPR(l), rws, 0); if constexpr (chk_at(T + 2) == NB - 2) ld8_sc1(pollz, voFZ(l), rws, 0); }
    }
    constexpr int cu = cr_unit(T), ck = cr_k(T);
    if constexpr (cu >= 0) hown[ck] = *(LAS u32x4*)(size_t)(aHO + (ck & 1) * 4096);
  };
  auto chunk_store = [&](auto Tc) {
    constexpr int T = decltype(Tc)::value, cu = cr_unit(T), ck = cr_k(T);
    if constexpr (cu >= 0) __builtin_amdgcn_raw_buffer_store_b128(hown[ck], rws, voH + ck * 1024, hbase_own + cu * 16384, 16);
  };
  const unsigned pbase = WS_PART + (unsigned)(i * 3 + jn) * (16 * 12288) + rg * 3072;
  u32x4 pkeep[9];
  auto pstore = [&](auto Cc, auto Nc) {
    constexpr int c = decltype(Cc)::value, n = decltype(Nc)::value;
    const f32x16 &a = acc2[2 * c], &d = acc2[2 * c + 1];
    const bool up = n >= s;
    auto pick = [&](const f32x16& v, int e) { return up ? v[4 * (n + 1) + e] : v[4 * n + e]; };
    const u32x4 va = {pk2(pick(a, 0), pick(a, 1)), pk2(pick(a, 2), pick(a, 3)), pk2(pick(d, 0), pick(d, 1)), pk2(pick(d, 2), pick(d, 3))};
    pkeep[3 * c + n] = va;
    __builtin_amdgcn_raw_buffer_store_b128(pkeep[3 * c + n], rws, voH + c * 1024, pbase - rg * 4096 + ((n + (up ? 1 : 0)) * 4 + s) * 12288, 16);
  };
  mkW2c(lane);
  xphase(std::true_type{}, [&]() { syncC(IC<9>{}); });
  progv = u32x2{(unsigned)base, (unsigned)(base >> 32)};
  STAMP(2);
  sfor<9, NST>([&](auto Tc) {
    constexpr int T = decltype(Tc)::value, b = blk_of(T), part = b >= 0 ? T - bstart(b) : 0;
    if constexpr (T < 12) {
      if constexpr (T == 9) aug(IC<1>{}, IC<1>{});
      allsites(Tc, std::true_type{});
      sfor<0, 8>([&](auto Ic) { epigap(IC<1>{}, IC<1>{}, IC<8 * (T - 9) + decltype(Ic)::value>{}, IC<1>{}, IC<4>{}, IC<7>{}); });
      if constexpr (T == 11) {
#pragma unroll
        for (int k = 0; k < 4; ++k) hown[k] = hv[k];
        base2(IC<0>{}); sfor<0, PF>([&](auto Ic) { pf[decltype(Ic)::value] = rd2(IC<0>{}, Ic); });
      }
      syncC(IC<T + 1>{});
    } else if constexpr (b < 0) {
      allsites(Tc, std::true_type{});
      issueH(Tc, IC<0>{}); issueH(Tc, IC<1>{});
      chunk_store(Tc);
      if constexpr (blk_of(T + 1) >= 0) { base2(IC<blk_of(T + 1)>{}); sfor<0, PF>([&](auto Ic) { pf[decltype(Ic)::value] = rd2(IC<blk_of(T + 1)>{}, Ic); }); }
      syncC(IC<T + 1>{});
    } else {
      constexpr bool nextblk = part == 2 && blk_of(T + 1) >= 0;
      if constexpr (nextblk) base2(IC<b + 1>{});
      __builtin_amdgcn_sched_barrier(0);
      sfor<0, 8>([&](auto Ic) {
        constexpr int I = decltype(Ic)::value, G = 8 * part + I, ks = g_ks(b, G), tn = g_tn(b, G);
        const bf16x8 cur = pf[I % PF];
        if constexpr (G + PF < 24) pf[I % PF] = rd2(IC<b>{}, IC<G + PF>{});
        else if constexpr (nextblk) pf[I % PF] = rd2(IC<b + 1>{}, IC<G + PF - 24>{});
        u32x4 hb;
        if constexpr (bsel(b) == 0) hb = hown[ks]; else hb = hring[kRing[b] < 0 ? 0 : kRing[b]][ks];
        acc2[tn] = __builtin_amdgcn_mfma_f32_32x32x16_bf16(cur, __builtin_bit_cast(bf16x8, hb), (b == 0 && ks == 0) ? zero16 : acc2[tn], 0, 0, 0);
        __builtin_amdgcn_sched_barrier(0);
        if constexpr (I == 0) site(Tc, IC<0>{}, std::true_type{});
        if constexpr (I == 2) site(Tc, IC<1>{}, std::true_type{});
        if constexpr (I == 1) issueH(Tc, IC<0>{});
        if constexpr (I == 3) issueH(Tc, IC<1>{});
        if constexpr (I == 4) site(Tc, IC<2>{}, std::true_type{});
        if constexpr (I == 5) chunk_store(Tc);
        if constexpr (I == 6) site(Tc, IC<3>{}, std::true_type{});
        if constexpr (b == NB - 1 && part >= 1 && I >= 2 && I <= 4) pstore(IC<part - 1>{}, IC<I - 2>{});
        if constexpr (I == 7) {
          asm volatile("" : "+v"(acc2[0]), "+v"(acc2[1]), "+v"(acc2[2]), "+v"(acc2[3]), "+v"(acc2[4]), "+v"(acc2[5]));
          if constexpr (T + 1 < NST) syncC(IC<T + 1>{});
        }
        __builtin_amdgcn_sched_barrier(0);
      });
    }
  });
  asm volatile("" :: "v"(progv), "v"(proga));
  STAMP(4);
  TLDUMP();
  const int tl = flane(), tm = tl & 31, thh = tl >> 5;
  const int pos = 128 * i + 32 * rg + tm;
  pstore(IC<2>{}, IC<0>{}); pstore(IC<2>{}, IC<1>{}); pstore(IC<2>{}, IC<2>{});
  float pre[24];
  {
    float own[24];
    sfor<0, 4>([&](auto Gc) { constexpr int g = decltype(Gc)::value;
      if (g == s) {
#pragma unroll
        for (int tn = 0; tn < 6; ++tn)
#pragma unroll
          for (int e = 0; e < 4; ++e) own[4 * tn + e] = acc2[tn][4 * g + e];
      } });
    const float rstd = 1.0f / sd;
#pragma unroll
    for (int tn = 0; tn < 6; ++tn) {
      const int cl = 8 * tn + 4 * thh;
      const s16x4 zt = __builtin_amdgcn_ds_read_tr16_b64_v4i16((LAS s16x4*)(lds + L_Z + (cl + ((tl & 15) >> 2)) * 256 + (32 * rg + 16 * ((tl >> 4) & 1) + 4 * (tl & 3)) * 2));
      const u32x2 bv = *(LAS u32x2*)(lds + L_B2RAW + (48 * s + cl) * 2);
      const u32x2 xv = *(LAS u32x2*)(lds + L_XF + (32 * rg + tm) * 96 + cl * 2);
#pragma unroll
      for (int e = 0; e < 4; ++e) {
        const float xe = (e & 1) ? bhi(xv[e >> 1]) : blo(xv[e >> 1]);
        const float be_ = (e & 1) ? bhi(bv[e >> 1]) : blo(bv[e >> 1]);
        const float xn = rb((xe - mean_f) * rstd);
        pre[4 * tn + e] = __builtin_fmaf(xn, b2f((u16)zt[e]), xe) + (own[4 * tn + e] + be_);
      }
    }
  }
  asm volatile("s_waitcnt vmcnt(0)" :: "v"(pkeep[0]), "v"(pkeep[1]), "v"(pkeep[2]), "v"(pkeep[3]), "v"(pkeep[4]), "v"(pkeep[5]), "v"(pkeep[6]), "v"(pkeep[7]), "v"(pkeep[8]) : "memory");
  if (tl == 0) flag_store(p.ws, WS_PFL + (((i * 3 + jn) * 4 + s) * 4 + rg) * FL_STRIDE, base + 1);
  STAMP(5);
  spin_flags(p.ws, rws, WS_PFL + (((i * 3 + jn) * 4 + (tl & 3)) * 4 + rg) * FL_STRIDE, tl < 4 && tl != s, base, 1);
  STAMP(6);
  u32x4 pa[3][3];
#pragma unroll
  for (int o = 0; o < 3; ++o) { const int so = o + (o >= s ? 1 : 0);
#pragma unroll
    for (int c = 0; c < 3; ++c) ld16_sc1_s(pa[o][c], tl * 16 + c * 1024, rws, pbase + (s * 4 + so) * 12288); }
  asm volatile("s_waitcnt vmcnt(0)" : "+v"(pa[0][0]), "+v"(pa[0][1]), "+v"(pa[0][2]), "+v"(pa[1][0]), "+v"(pa[1][1]), "+v"(pa[1][2]), "+v"(pa[2][0]), "+v"(pa[2][1]), "+v"(pa[2][2]) :: "memory");
  STAMP(8);
#pragma unroll
  for (int tn = 0; tn < 6; ++tn) {
    float o[4];
#pragma unroll
    for (int e = 0; e < 4; ++e) {
      float sum = pre[4 * tn + e];
#pragma unroll
      for (int oo = 0; oo < 3; ++oo) { const unsigned wd = pa[oo][tn >> 1][2 * (tn & 1) + (e >> 1)]; sum += (e & 1) ? bhi(wd) : blo(wd); }
      o[e] = sum;
    }
    const u32x2 ov = {pk2(o[0], o[1]), pk2(o[2], o[3])};
    *(GAS u32x2*)(p.out + (size_t)pos * D_ + 192 * jn + 48 * s + 8 * tn + 4 * thh) = ov;
  }
  STAMP(7);
}

constexpr unsigned A_CH = 12928;
constexpr unsigned A_H0 = 32, A_H1 = A_H0 + 4096 + 64, A_X = A_H1 + 4096 + 512  ;
static_assert(A_X + 4096 <= A_CH && A_X % 16 == 0, "channel image");
typedef unsigned u32x4a4 __attribute__((ext_vector_type(4), aligned(4)));
typedef float f32x4v __attribute__((ext_vector_type(4)));

constexpr unsigned A_STG = 9 * A_CH;
constexpr unsigned A_CSP = A_STG;
constexpr unsigned A_RK = 0, A_RV = 40960;
constexpr unsigned A_JUNK = A_STG + 288 * 144;
static_assert(A_JUNK + 4096 <= 163840, "aux LDS");

DI void aux_role(const Params& p, LAS char* lds, const int a, const int al, const int cset, u32x2 epv) {
  u64 base;
  const int tid = threadIdx.x, lane = tid & 63, wave = rfl(tid >> 6);
  const rsrc_t rws = mkrsrc(p.ws, WS_END);
  const unsigned ldsb = (unsigned)(size_t)lds;
  STAMP(0);
  constexpr unsigned OOB = 0x7fffff00u;
  const rsrc_t rw1 = mkrsrc(p.w1, D_ * F_ * 2);
  u32x4 wv[11];
  const int r7 = tid / 72, c72 = tid - 72 * r7;
#pragma unroll
  for (int k = 0; k < 11; ++k) {
    const int rl = r7 + 7 * k;
    wv[k] = __builtin_amdgcn_raw_buffer_load_b128(rw1, (r7 < 7 && rl < 72) ? (unsigned)((72 * al + rl) * 9216 + (576 * cset) * 2 + c72 * 16) : OOB, 0, 0);
  }
  const int pt = a >> 1, hf = a & 1;
  const rsrc_t rk = mkrsrc(p.k, S_ * D_ * 2), rv = mkrsrc(p.v, S_ * D_ * 2), rs = mkrsrc(p.shk, D_ * 8), rh = mkrsrc(p.ssk, D_ * S_ * 2);
#pragma unroll
  for (int n = 0; n < 5; ++n) {
    const unsigned L = (wave + 8 * n) * 1024 + lane * 16, row = L / 576, col = L - 576 * row;
    const int sr = 64 * pt - 3 + (int)row;
    dma16(ldsb + A_RK + rfl(wave + 8 * n) * 1024, (row < 67 && sr >= 0) ? (unsigned)(sr * 1152) + col : OOB, rk, 576 * hf);
    dma16(ldsb + A_RV + rfl(wave + 8 * n) * 1024, row < 64 ? (unsigned)((64 * pt + row) * 1152) + col : OOB, rv, 576 * hf);
  }
  u32x4 sh[3];
#pragma unroll
  for (int n = 0; n < 3; ++n) { const int it = tid + 512 * n, cp = it % 144; sh[n] = __builtin_amdgcn_raw_buffer_load_b128(rs, (288 * hf + 2 * cp) * 8, 0, 0); }
  {
    float cs[8] = {0.f, 0.f, 0.f, 0.f, 0.f, 0.f, 0.f, 0.f};
#pragma unroll
    for (int k = 0; k < 11; ++k)
#pragma unroll
      for (int e = 0; e < 4; ++e) { cs[2 * e] += blo(wv[k][e]); cs[2 * e + 1] += bhi(wv[k][e]); }
    if (r7 < 7) { LAS char* dst = lds + A_CSP + (r7 * 576 + c72 * 8) * 4;
      *(LAS f32x4*)dst = f32x4{cs[0], cs[1], cs[2], cs[3]}; *(LAS f32x4*)(dst + 16) = f32x4{cs[4], cs[5], cs[6], cs[7]}; }
    base = take_epoch(p.ws, epv);
    sbar();
    if (tid < 144) {
      f32x4 t = *(LAS f32x4*)(lds + A_CSP + tid * 16);
#pragma unroll
      for (int r = 1; r < 7; ++r) { const f32x4 u = *(LAS f32x4*)(lds + A_CSP + r * 2304 + tid * 16); t = f32x4{t[0] + u[0], t[1] + u[1], t[2] + u[2], t[3] + u[3]}; }
      __builtin_amdgcn_raw_buffer_store_b128(__builtin_bit_cast(u32x4, t), rws, WS_CS + (al * F_ + 576 * cset) * 4 + tid * 16, 0, 16);
    }
  }
  STAMP(6);
  asm volatile("s_waitcnt vmcnt(0) lgkmcnt(0)\n\ts_barrier" : "+v"(sh[0]), "+v"(sh[1]), "+v"(sh[2]) :: "memory");
  if (tid == 0) flag_store(p.ws, WS_FA + (cset * 8 + al) * FL_STRIDE, base + 1);
  STAMP(2);
  unsigned touch[2];
  {
    const rsrc_t rw2 = mkrsrc(p.w2, F_ * D_ * 2);
#pragma unroll
    for (int k = 0; k < 2; ++k) { const int ln = tid + 512 * k;
      asm volatile("buffer_load_dword %0, %1, %2, %3 offen" : "=v"(touch[k]) : "v"(ln < 648 ? (unsigned)(ln * 128) : OOB), "s"(rw2), "s"((unsigned)((576 * cset + 72 * al) * 1152)) : "memory"); }
  }
  u32x4 kvo[3][2];
#pragma unroll
  for (int n = 0; n < 3; ++n) {
    const int it = (tid + 512 * n) < 1152 ? tid + 512 * n : tid, o = it / 144, cp = it - 144 * o;
    unsigned kk[11], vv[8];
#pragma unroll
    for (int r = 0; r < 11; ++r) kk[r] = *(LAS unsigned*)(lds + A_RK + (8 * o + r) * 576 + cp * 4);
#pragma unroll
    for (int r = 0; r < 8; ++r) vv[r] = *(LAS unsigned*)(lds + A_RV + (8 * o + r) * 576 + cp * 4);
    float o0[8], o1[8];
#pragma unroll
    for (int r = 0; r < 8; ++r) {
      float a0 = blo(kk[r]) * bhi(sh[n][1]); a0 = __builtin_fmaf(blo(kk[r + 1]), blo(sh[n][1]), a0); a0 = __builtin_fmaf(blo(kk[r + 2]), bhi(sh[n][0]), a0); a0 = __builtin_fmaf(blo(kk[r + 3]), blo(sh[n][0]), a0);
      float a1 = bhi(kk[r]) * bhi(sh[n][3]); a1 = __builtin_fmaf(bhi(kk[r + 1]), blo(sh[n][3]), a1); a1 = __builtin_fmaf(bhi(kk[r + 2]), bhi(sh[n][2]), a1); a1 = __builtin_fmaf(bhi(kk[r + 3]), blo(sh[n][2]), a1);
      o0[r] = a0 * blo(vv[r]); o1[r] = a1 * bhi(vv[r]);
    }
    kvo[n][0] = u32x4{pk2(o0[7], o0[6]), pk2(o0[5], o0[4]), pk2(o0[3], o0[2]), pk2(o0[1], o0[0])};
    kvo[n][1] = u32x4{pk2(o1[7], o1[6]), pk2(o1[5], o1[4]), pk2(o1[3], o1[2]), pk2(o1[1], o1[0])};
  }
#pragma unroll
  for (int n = 0; n < 3; ++n) {
    const int it = tid + 512 * n, o = it / 144, cp = it - 144 * o;
    if (it < 1152) { LAS char* dst = lds + A_STG + (2 * cp) * 144 + o * 16; *(LAS u32x4*)dst = kvo[n][0]; *(LAS u32x4*)(dst + 144) = kvo[n][1]; }
  }
  STAMP(8);
  u32x4 hA[5]; unsigned hB[5];
#pragma unroll
  for (int n = 0; n < 5; ++n) {
    const int un = wave + 8 * n, c = un >> 2, i = un & 3;
    const unsigned go = un < 36 ? (unsigned)((9 * a + c) * 4096 + i * 1024 + lane * 16) : OOB;
    asm volatile("buffer_load_dwordx4 %0, %2, %4, 0 offen\n\tbuffer_load_dword %1, %3, %4, 0 offen" : "=&v"(hA[n]), "=&v"(hB[n]) : "v"(go), "v"(un < 36 ? go + 16 : OOB), "s"(rh) : "memory");
  }
  sbar();
  if (tid < 9 * 35) { const int c = tid / 35, i = tid - 35 * c;
    const unsigned off = c * A_CH + (i < 2 ? i * 16 : i < 3 ? A_H1 - 32 : A_X - 512 + (i - 3) * 16);
    *(LAS u32x4*)(lds + off) = u32x4{0u, 0u, 0u, 0u}; }
#pragma unroll
  for (int n = 0; n < 5; ++n) {
    const int gch = wave + 8 * n;
    if (gch < 36) {
      const int ch = 8 * gch + (lane >> 3), o = lane & 7;
      const u32x4 v = *(LAS u32x4*)(lds + A_STG + ch * 144 + o * 16);
      __builtin_amdgcn_raw_buffer_store_b128(v, rws, WS_KVT + (288 * hf + ch) * 4096 + (64 * pt + 8 * o) * 2, 0, 16);
    }
  }
  asm volatile("s_waitcnt vmcnt(4)" : "+v"(hA[0]), "+v"(hA[1]), "+v"(hA[2]), "+v"(hA[3]), "+v"(hA[4]), "+v"(hB[0]), "+v"(hB[1]), "+v"(hB[2]), "+v"(hB[3]), "+v"(hB[4]) :: "memory");
  asm volatile("" : "+v"(touch[0]), "+v"(touch[1]));
#pragma unroll
  for (int n = 0; n < 5; ++n) {
    const int un = wave + 8 * n, c = un >> 2, i = un & 3;
    if (un < 36) {
      const u32x4 A = hA[n];
      *(LAS u32x4*)(lds + c * A_CH + A_H0 + i * 1024 + lane * 16) = A;
      *(LAS u32x4*)(lds + c * A_CH + A_H1 + i * 1024 + lane * 16) =
          u32x4{__builtin_amdgcn_alignbit(A[1], A[0], 16), __builtin_amdgcn_alignbit(A[2], A[1], 16), __builtin_amdgcn_alignbit(A[3], A[2], 16), __builtin_amdgcn_alignbit(hB[n], A[3], 16)};
      if (i == 0 && lane == 0) *(LAS u32x4*)(lds + c * A_CH + A_H1 - 16) = u32x4{0u, 0u, 0u, A[0] << 16};
    }
  }
  STAMP(5);
  publish_flag(p.ws, WS_FB + a * FL_STRIDE, base + 1);
  STAMP(1);
  {
    const int hfc = (9 * a) >= 288 ? 1 : 0;
    if (wave == 0) {
      spin_flags(p.ws, rws, WS_FB + (2 * (lane & 31) + hfc) * FL_STRIDE, lane < 32, base, 1);
      __builtin_amdgcn_fence(__ATOMIC_ACQUIRE, "agent");
      asm volatile("s_waitcnt vmcnt(0)" ::: "memory");
    }
    sbar();
#pragma unroll
    for (int n = 0; n < 5; ++n) {
      const int un = wave + 8 * n, c = un >> 2, i = un & 3;
      if (un < 36) dma16(rfl(ldsb + c * A_CH + A_X + i * 1024), lane * 16, rws, rfl(WS_KVT + (9 * a + c) * 4096 + i * 1024));
    }
    asm volatile("s_waitcnt vmcnt(0) lgkmcnt(0)\n\ts_barrier" ::: "memory");
  }
  STAMP(3);
  const int b = lane & 15, g = lane >> 4;
  const int odd = (b + 1) & 1;
  const unsigned aH = (odd ? A_H1 : A_H0) + 2 * (9 + b - 8 * g - odd);
  const unsigned aX = A_X + 32 * b - 32 + 16 * g;
  auto rdH = [&](unsigned chb, int mp) -> bf16x8 { return __builtin_bit_cast(bf16x8, (u32x4)*(LAS u32x4a4*)(lds + chb + aH + 64 * mp)); };
  auto rdX = [&](unsigned chb, int n, int kappa) -> bf16x8 { return *(LAS bf16x8*)(lds + chb + aX + 512 * n - 64 * kappa); };
  const float dd0 = b2f(p.ssd[9 * a + wave]), dd8 = b2f(p.ssd[9 * a + 8]);
  auto finish = [&](int c, int t, f32x4v y) {
    const u32x2 kvr = *(LAS u32x2*)(lds + c * A_CH + A_X + (32 * t + 2 * b + (g >> 1)) * 16 + ((g & 1) ? 0 : 8));
    const float dd = c == 8 ? dd8 : dd0;
    const float z0 = rb(y[0]) + rb(bhi(kvr[1]) * dd), z1 = rb(y[1]) + rb(blo(kvr[1]) * dd), z2 = rb(y[2]) + rb(bhi(kvr[0]) * dd), z3 = rb(y[3]) + rb(blo(kvr[0]) * dd);
    __builtin_amdgcn_raw_buffer_store_b64(u32x2{pk2(z0, z1), pk2(z2, z3)}, rws, WS_ZT + (9 * a + c) * 4096 + (256 * t + 16 * b + 4 * g) * 2, 0, 16);
  };
  {
    f32x4v acc[8];
#pragma unroll
    for (int t = 0; t < 8; ++t) acc[t] = f32x4v{0.f, 0.f, 0.f, 0.f};
    unsigned bH[4], bX = ldsb + wave * A_CH + aX;
#pragma unroll
    for (int q4 = 0; q4 < 4; ++q4) bH[q4] = ldsb + wave * A_CH + aH + 1024 * q4;
    auto ldH = [&](int m) -> bf16x8 { return __builtin_bit_cast(bf16x8, (u32x4)*(LAS u32x4a4*)(size_t)(bH[m >> 1] + 512 * (m & 1))); };
    auto ldX = [&](int n) -> bf16x8 { return *(LAS bf16x8*)(size_t)(bX + 512 * n); };
    auto step = [&]() {
#pragma unroll
      for (int q4 = 0; q4 < 4; ++q4) bH[q4] += 64;
      bX -= 64;
      asm volatile("" : "+v"(bH[0]), "+v"(bH[1]), "+v"(bH[2]), "+v"(bH[3]), "+v"(bX));
    };
    bf16x8 H[2][8], X[2][8];
    asm volatile("" : "+v"(bH[0]), "+v"(bH[1]), "+v"(bH[2]), "+v"(bH[3]), "+v"(bX));
#pragma unroll
    for (int m = 0; m < 8; ++m) { H[0][m] = ldH(m); X[0][m] = ldX(m); }
#pragma unroll 1
    for (int kp = 0; kp < 4; ++kp) {
#pragma unroll
      for (int hb = 0; hb < 2; ++hb) {
        step();
        __builtin_amdgcn_sched_barrier(0);
#pragma unroll
        for (int m = 0; m < 8; ++m) {
          H[hb ^ 1][m] = ldH(m); X[hb ^ 1][m] = ldX(m);
#pragma unroll
          for (int t = m; t < 8; ++t) acc[t] = __builtin_amdgcn_mfma_f32_16x16x32_bf16(H[hb][m], X[hb][t - m], acc[t], 0, 0, 0);
          __builtin_amdgcn_sched_barrier(0);
        }
      }
    }
#pragma unroll
    for (int t = 0; t < 8; ++t) finish(wave, t, acc[t]);
  }
  STAMP(9);
  {
    const unsigned chb = 8 * A_CH;
    f32x4v acc0 = {0.f, 0.f, 0.f, 0.f}, acc1 = acc0;
    const int t = wave;
    bf16x8 H[2][4], X[2][4];
    auto rd4 = [&](int hb, int g4) {
#pragma unroll
      for (int k = 0; k < 4; ++k) { H[hb][k] = rdH(chb + 256 * g4, k); X[hb][k] = *(LAS bf16x8*)(lds + chb + aX + 512 * t - 256 * g4 - 64 * k); }
    };
    rd4(0, 0);
#pragma unroll 1
    for (int g8 = 0; g8 <= t; ++g8) {
      rd4(1, 2 * g8 + 1);
      __builtin_amdgcn_sched_barrier(0);
#pragma unroll
      for (int k = 0; k < 4; k += 2) { acc0 = __builtin_amdgcn_mfma_f32_16x16x32_bf16(H[0][k], X[0][k], acc0, 0, 0, 0); acc1 = __builtin_amdgcn_mfma_f32_16x16x32_bf16(H[0][k + 1], X[0][k + 1], acc1, 0, 0, 0); }
      __builtin_amdgcn_sched_barrier(0);
      rd4(0, g8 < t ? 2 * g8 + 2 : 0);
      __builtin_amdgcn_sched_barrier(0);
#pragma unroll
      for (int k = 0; k < 4; k += 2) { acc0 = __builtin_amdgcn_mfma_f32_16x16x32_bf16(H[1][k], X[1][k], acc0, 0, 0, 0); acc1 = __builtin_amdgcn_mfma_f32_16x16x32_bf16(H[1][k + 1], X[1][k + 1], acc1, 0, 0, 0); }
      __builtin_amdgcn_sched_barrier(0);
    }
    finish(8, t, acc0 + acc1);
  }
  publish_flag(p.ws, WS_FZ + a * FL_STRIDE, base + 1);
  STAMP(4);
  if (a == 63 && wave == 0) {
#pragma unroll 1
    for (int k = 0; k < 4; ++k) spin_flags(p.ws, rws, WS_ARR + (64 * k + lane) * FL_STRIDE, true, base, 0);
    if (lane == 0) flag_store(p.ws, WS_EPOCH, base >> 4);
  }
}

__global__ __launch_bounds__(512, 2) void k_main(Params p) {
  extern __shared__ __attribute__((aligned(16))) char smem[];
  LAS char* lds = (LAS char*)smem;
  const int b = blockIdx.x, g = b & 7, qq = b >> 3;
  u32x2 epv;
  asm volatile("global_load_dwordx2 %0, %1, %2 sc1" : "=v"(epv) : "v"(0u), "s"(p.ws) : "memory");
  if (qq < 24) mlp_role(p, lds, 8 * (g & 1) + qq / 3, g >> 1, qq % 3, epv);
  else aux_role(p, lds, (qq - 24) * 8 + g, qq - 24, g, epv);
}
}
#if defined(__HIP_DEVICE_COMPILE__)
#pragma clang attribute pop
#endif

extern "C" void kernel_launch(void* const* d_in, const int* in_sizes, int n_in, void* d_out, int out_size, void* d_ws, size_t ws_size, hipStream_t stream) {
  using namespace mk;
  Params p;
  p.x = (const u16*)d_in[0]; p.k = (const u16*)d_in[1]; p.v = (const u16*)d_in[2]; p.shk = (const u16*)d_in[3]; p.ssk = (const u16*)d_in[4]; p.ssd = (const u16*)d_in[5];
  p.w1 = (const u16*)d_in[7]; p.b1 = (const u16*)d_in[8]; p.w2 = (const u16*)d_in[9]; p.b2 = (const u16*)d_in[10];
  p.out = (u16*)d_out; p.ws = (char*)d_ws;
  (void)hipFuncSetAttribute((const void*)k_main, hipFuncAttributeMaxDynamicSharedMemorySize, LDS_BYTES);
  k_main<<<256, 512, LDS_BYTES, stream>>>(p);
}
```

```cpp
#include <hip/hip_runtime.h>
#include <stdint.h>
#include <type_traits>

#if defined(__HIP_DEVICE_COMPILE__)
#pragma clang attribute push(__attribute__((target("no-packed-fp32-ops"))), apply_to = function)
#endif
namespace mk {
constexpr int S_ = 2048, D_ = 576, F_ = 4608;
typedef unsigned short u16;
typedef unsigned long long u64;
typedef __bf16 bf16x8 __attribute__((ext_vector_type(8)));
typedef __bf16 bf16x2 __attribute__((ext_vector_type(2)));
typedef short s16x4 __attribute__((ext_vector_type(4)));
typedef short s16x8 __attribute__((ext_vector_type(8)));
typedef float f32x16 __attribute__((ext_vector_type(16)));
typedef float f32x4 __attribute__((ext_vector_type(4)));
typedef float f32x2 __attribute__((ext_vector_type(2)));
typedef unsigned u32x4 __attribute__((ext_vector_type(4)));
typedef unsigned u32x2 __attribute__((ext_vector_type(2)));
#define LAS __attribute__((address_space(3)))
#define GAS __attribute__((address_space(1)))
#define DI __device__ __forceinline__
typedef __amdgpu_buffer_rsrc_t rsrc_t;

constexpr unsigned FL_STRIDE = 128;
constexpr unsigned WS_EPOCH = 0;
constexpr unsigned WS_ARR   = 1024;
constexpr unsigned WS_PROG  = WS_ARR + 256 * FL_STRIDE;
constexpr unsigned WS_PFL   = WS_PROG + 192 * FL_STRIDE;
constexpr unsigned WS_FA    = WS_PFL + 768 * FL_STRIDE;
constexpr unsigned WS_FB    = WS_FA + 64 * FL_STRIDE;
constexpr unsigned WS_FZ    = WS_FB + 64 * FL_STRIDE;
constexpr unsigned WS_TMO   = WS_FZ + 64 * FL_STRIDE;
constexpr unsigned WS_FLAGS_END = 262144;
static_assert(WS_TMO + 128 <= WS_FLAGS_END, "flags");
constexpr unsigned WS_CS    = WS_FLAGS_END;
constexpr unsigned WS_KVT   = WS_CS + 8 * F_ * 4;
constexpr unsigned WS_ZT    = WS_KVT + D_ * S_ * 2;
constexpr unsigned WS_H     = WS_ZT + D_ * S_ * 2;
constexpr unsigned WS_PART  = WS_H + S_ * F_ * 2;
constexpr unsigned WS_STAMP = WS_PART + 48 * 16 * 12288;
constexpr unsigned WS_TL    = WS_STAMP + 256 * 64 * 8;
constexpr unsigned WS_END   = WS_TL + 256 * 1024 * 8;

struct Params {
  const u16 *x, *k, *v, *shk, *ssk, *ssd, *w1, *b1, *w2, *b2;
  u16* out; char* ws;
};

template <int I> using IC = std::integral_constant<int, I>;
template <int B, int E, class F> DI void sfor(F&& f) { if constexpr (B < E) { f(IC<B>{}); sfor<B + 1, E>(f); } }

DI float asf(unsigned u) { return __builtin_bit_cast(float, u); }
DI float b2f(u16 u) { return asf((unsigned)u << 16); }
DI float blo(unsigned u) { return asf(u << 16); }
DI float bhi(unsigned u) { return asf(u & 0xffff0000u); }
DI unsigned pk2(float a, float b) { f32x2 v = {a, b}; return __builtin_bit_cast(unsigned, __builtin_convertvector(v, bf16x2)); }
DI float rb(float f) { return blo(pk2(f, 0.f)); }
DI rsrc_t mkrsrc(const void* p, unsigned bytes) { return __builtin_amdgcn_make_buffer_rsrc((void*)p, 0, bytes, 0x00020000); }
DI unsigned rfl(unsigned v) { return __builtin_amdgcn_readfirstlane(v); }

DI void dma16(unsigned ldsaddr, unsigned voff, rsrc_t srd, unsigned soff) {
  asm volatile("s_mov_b32 m0, %0\n\ts_nop 0\n\tbuffer_load_dwordx4 %1, %2, %3 offen lds" :: "s"(ldsaddr), "v"(voff), "s"(srd), "s"(soff) : "memory");
}
template <unsigned MI, unsigned SI> DI void dma16i(unsigned mb, unsigned voff, rsrc_t srd, unsigned sb) {
  unsigned tmp;
  asm volatile("s_add_u32 m0, %1, %5\n\ts_add_u32 %0, %4, %6\n\tbuffer_load_dwordx4 %2, %3, %0 offen lds"
               : "=&s"(tmp) : "s"(mb), "v"(voff), "s"(srd), "s"(sb), "i"(MI), "i"(SI) : "scc", "memory");
}
template <unsigned SI> DI void ld16i_sc1(u32x4& dst, unsigned voff, rsrc_t srd, unsigned sb) {
  unsigned tmp;
  asm volatile("s_add_u32 %1, %4, %5\n\tbuffer_load_dwordx4 %0, %2, %3, %1 offen sc1" : "=v"(dst), "=&s"(tmp) : "v"(voff), "s"(srd), "s"(sb), "i"(SI) : "scc", "memory");
}
template <int WHICH> DI void dma16_if(unsigned sel, unsigned ldsaddr, unsigned voff, rsrc_t srd, unsigned soff) {
  asm volatile("s_cmp_lg_u32 %4, %5\n\ts_cbranch_scc1 .Ldma%=\n\ts_mov_b32 m0, %0\n\ts_nop 0\n\tbuffer_load_dwordx4 %1, %2, %3 offen lds\n.Ldma%=:"
               :: "s"(ldsaddr), "v"(voff), "s"(srd), "s"(soff), "s"(sel), "i"(WHICH) : "scc", "memory");
}
DI void ld16_sc1(u32x4& dst, unsigned voff, rsrc_t srd, unsigned soff) {
  asm volatile("buffer_load_dwordx4 %0, %1, %2, %3 offen sc1" : "=v"(dst) : "v"(voff), "s"(srd), "s"(soff) : "memory");
}
DI void ld16_sc1_s(u32x4& dst, unsigned voff, rsrc_t srd, unsigned soff) {
  asm volatile("s_nop 4\n\tbuffer_load_dwordx4 %0, %1, %2, %3 offen sc1" : "=v"(dst) : "v"(voff), "s"(srd), "s"(rfl(soff)) : "memory");
}
DI void ld8_sc1_s(u32x2& dst, unsigned voff, rsrc_t srd, unsigned soff) {
  asm volatile("s_nop 4\n\tbuffer_load_dwordx2 %0, %1, %2, %3 offen sc1" : "=v"(dst) : "v"(voff), "s"(srd), "s"(rfl(soff)) : "memory");
}
DI void ld8_sc1(u32x2& dst, unsigned voff, rsrc_t srd, unsigned soff) {
  asm volatile("buffer_load_dwordx2 %0, %1, %2, %3 offen sc1" : "=v"(dst) : "v"(voff), "s"(srd), "s"(soff) : "memory");
}
DI void ld4_sc1(unsigned& dst, unsigned voff, rsrc_t srd, unsigned soff) {
  asm volatile("buffer_load_dword %0, %1, %2, %3 offen sc1" : "=v"(dst) : "v"(voff), "s"(srd), "s"(soff) : "memory");
}
template <int N> DI void waitv() { asm volatile("s_waitcnt vmcnt(%0)" :: "i"(N) : "memory"); }
DI void bar() { __builtin_amdgcn_sched_barrier(0); asm volatile("s_barrier" ::: "memory"); __builtin_amdgcn_sched_barrier(0); }
DI void lgkm0() { asm volatile("s_waitcnt lgkmcnt(0)" ::: "memory"); }

DI int flane() { int l; asm volatile("v_mbcnt_lo_u32_b32 %0, -1, 0\n\tv_mbcnt_hi_u32_b32 %0, -1, %0" : "=v"(l)); return l; }
DI bool wall(bool c) { return __builtin_amdgcn_ballot_w64(!c) == 0; }
DI bool flag_ok(u32x2 v, u64 base, unsigned need) { u64 d = (((u64)v.y << 32) | v.x) - base; return d >= need && d <= 15; }
DI void flag_store(char* ws, unsigned off, u64 val) { __hip_atomic_store((GAS u64*)(ws + off), val, __ATOMIC_RELAXED, __HIP_MEMORY_SCOPE_AGENT); }
DI void spin_flags(char* ws, rsrc_t wsr, unsigned voff, bool active, u64 base, unsigned need) {
  const unsigned tag = (unsigned)base | 1u;
  for (unsigned it = 0;; ++it) {
    u32x2 v; ld8_sc1(v, voff, wsr, 0); asm volatile("s_waitcnt vmcnt(0)" : "+v"(v) :: "memory");
    if (wall(!active || flag_ok(v, base, need))) return;
    if (it > 30000u) { __hip_atomic_store((GAS unsigned*)(ws + WS_TMO), tag, __ATOMIC_RELAXED, __HIP_MEMORY_SCOPE_AGENT); return; }
    if ((it & 255u) == 255u && rfl(__hip_atomic_load((GAS unsigned*)(ws + WS_TMO), __ATOMIC_RELAXED, __HIP_MEMORY_SCOPE_AGENT)) == tag) return;
    __builtin_amdgcn_s_sleep(1);
  }
}
DI void sbar() { asm volatile("s_waitcnt lgkmcnt(0)\n\ts_barrier" ::: "memory"); }
DI void publish_flag(char* ws, unsigned off, u64 val) {
  asm volatile("s_waitcnt vmcnt(0) lgkmcnt(0)\n\ts_barrier" ::: "memory");
  if (threadIdx.x == 0) flag_store(ws, off, val);
}
DI u64 take_epoch(char* ws, const u32x2& epv) {
  unsigned lo, hi;
  asm volatile("v_readfirstlane_b32 %0, %2\n\tv_readfirstlane_b32 %1, %3\n\ts_nop 3" : "=s"(lo), "=s"(hi) : "v"(epv.x), "v"(epv.y) : "memory");
  const u64 base = ((((u64)hi << 32) | lo) + 1) << 4;
  if (threadIdx.x == 0) flag_store(ws, WS_ARR + blockIdx.x * FL_STRIDE, base);
  return base;
}
DI float wave_xor_add8(float v) {
  v += __builtin_bit_cast(float, __builtin_amdgcn_update_dpp(0, __builtin_bit_cast(int, v), 0xB1, 0xf, 0xf, true));
  v += __builtin_bit_cast(float, __builtin_amdgcn_update_dpp(0, __builtin_bit_cast(int, v), 0x4E, 0xf, 0xf, true));
  v += __builtin_bit_cast(float, __builtin_amdgcn_update_dpp(0, __builtin_bit_cast(int, v), 0x141, 0xf, 0xf, true));
  return v;
}

constexpr unsigned R1 = 0;
constexpr unsigned R2 = 9 * 8192;
constexpr unsigned L_MISC = R2 + 73728;
constexpr unsigned L_AUG = L_MISC;
constexpr unsigned L_B1RAW = L_AUG + 6144;
constexpr unsigned L_B2RAW = L_B1RAW + 1024;
constexpr unsigned L_HO = L_B2RAW + 1024;
constexpr unsigned L_ST = R2 + 65536;
constexpr unsigned L_Z = R2;
constexpr unsigned L_XF = L_Z + 12288;
constexpr unsigned LDS_BYTES = 163840;

#define STAMP(n) do {} while (0)

#define TLK(T, K) do {} while (0)
#define TL(T) do {} while (0)
#define TLDUMP() do {} while (0)
namespace sch {
constexpr int NB = 18;
constexpr int TP = 36;
constexpr int kSel[NB]    = {    0,  0,  0,  1,  2,  0,  1,  2,  1,  2,  0,  1,  2,  0,  1,  2,  1,  2 };
constexpr int kUnit[NB]   = {    1,  0,  2,  1,  1,  3,  0,  0,  2,  2,  4,  3,  3,  5,  4,  4,  5,  5 };
constexpr int kStart[NB]  = {   12, 17, 20, 23, 26, 29, 32, 35, 38, 41, 44, 47, 50, 53, 56, 59, 62, 65 };
constexpr int kRing[NB]   = {   -1, -1, -1,  0,  1, -1,  2,  0,  1,  2, -1,  0,  1, -1,  2,  0,  1,  2 };
constexpr int kHIssue[NB] = {   -1, -1, -1, 20, 21, -1, 27, 29, 32, 35, -1, 41, 44, -1, 50, 53, 56, 59 };
constexpr int kW2Buf[NB]  = {    3,  4,  5,  3,  4,  5,  3,  4,  5,  0,  1,  2,  3,  4,  5,  0,  1,  2 };
constexpr int bsel(int b) { return kSel[b]; }
constexpr int bunit(int b) { return kUnit[b]; }
constexpr int bstart(int b) { return kStart[b]; }
constexpr int NST = bstart(NB - 1) + 3;
constexpr int TZ1 = 59, TZ2 = 65;
constexpr int g_ks(int b, int G) { return b == NB - 1 ? G % 4 : G / 6; }
constexpr int g_tn(int b, int G) { return b == NB - 1 ? G / 4 : G % 6; }
constexpr int blk_of(int T) { for (int b = 0; b < NB; ++b) if (T >= bstart(b) && T < bstart(b) + 3) return b; return -1; }
constexpr bool by_P(int u) { return u != 1 && u != 2; }
constexpr int gelu0(int u) { return u == 0 ? 9 : u == 5 ? TP : 9 * u - 9; }
constexpr int chunkW(int u, int k) { return u == 5 ? TP + 1 + k : gelu0(u) + 2 + 2 * k; }
constexpr int chunkR(int u, int k) { return chunkW(u, k) + 1; }
constexpr bool ho_written(int T) { for (int u = 0; u < 6; ++u) if (by_P(u)) for (int k = 0; k < 4; ++k) if (chunkW(u, k) == T) return true; return false; }
constexpr int cr_unit(int T) { for (int u = 0; u < 6; ++u) if (by_P(u)) for (int k = 0; k < 4; ++k) if (chunkR(u, k) == T) return u; return -1; }
constexpr int cr_k(int T) { for (int u = 0; u < 6; ++u) if (by_P(u)) for (int k = 0; k < 4; ++k) if (chunkR(u, k) == T) return k; return -1; }
constexpr int ownset(int u) { return (u == 1 || u == 2 || u == 4) ? 0 : 1; }
constexpr int pubK(int T) { return T == 16 ? 1 : T == 23 ? 2 : T == 25 ? 3 : T == 32 ? 4 : T == 41 ? 5 : T == 46 ? 6 : 0; }
constexpr int needOf(int u) { return u == 1 ? 1 : u == 0 ? 2 : u + 1; }
constexpr int TG2 = 12;
constexpr int c1_unit(int c) { return c < 27 ? c % 3 : 3 + (c - 27) / 9; }
constexpr int c1_kc(int c) { return c < 27 ? c / 3 : (c - 27) % 9; }
constexpr int c1_use(int c) { return c < 27 ? c / 3 : c - 18; }
#define W1LEAD 6
constexpr int c1_issue(int c) { return c < 9 ? -1 : c < 33 ? c / 3 - 2 : c < 37 ? 11 + (c - 33) / 2 : c - 18 - W1LEAD; }
constexpr int x_issue(int kc) { return kc < 4 ? -1 : kc - 3; }
constexpr int x_pos(int kc) { return (kc + 3) & 3; }
constexpr int w2_issue(int b) { return b == 0 ? 7 : b == 1 ? 11 : b <= 8 ? bstart(b) - 6 : bstart(b) - 9; }
constexpr int w2_buf(int b) { return kW2Buf[b]; }
constexpr int h_issue(int b) { return kHIssue[b]; }
constexpr bool dma_by_C(int T) { return T < 12; }
constexpr int nD(int T, bool C) {
  int n = 0;
  for (int c = 9; c < 54; ++c) if (c1_issue(c) == T) n += T < 9 ? 1 : (dma_by_C(T) == C) ? 2 : 0;
  for (int kc = 4; kc < 9; ++kc) if (x_issue(kc) == T) n += 2;
  for (int b = 0; b < NB; ++b) if (T >= w2_issue(b) && T < w2_issue(b) + 3) n += b == 0 ? 1 : (dma_by_C(T) == C) ? 2 : 0;
  return n;
}
constexpr int nH(int T) { int n = 0; for (int b = 0; b < NB; ++b) if (h_issue(b) >= 0 && (h_issue(b) == T || h_issue(b) + 1 == T)) n += 2; return n; }
constexpr int nS(int T) { return ((T >= 12 && cr_unit(T) >= 0) ? 1 : 0) + ((T == TG2 + 2 || T == TG2 + 4 || T == TG2 + 6 || T == TG2 + 8) ? 1 : 0); }
constexpr int cnt(int T, bool C) { return nD(T, C) + (C ? nH(T) + nS(T) : 0); }
constexpr int after(int t0, int T, bool C) { int n = 0; for (int t = t0 + 1; t < T; ++t) n += cnt(t, C); return n; }
constexpr int req(int T, bool C) {
  int r = -2;
  auto mx = [&r](int v) { r = r > v ? r : v; };
  for (int tt = T; tt <= T + 1; ++tt) {
    if (tt == T + 1 && T < 9) break;
    if (tt < 9) { mx(c1_issue(3 * tt)); mx(c1_issue(3 * tt + 1)); mx(c1_issue(3 * tt + 2)); mx(x_issue(tt)); }
    else {
      if (tt < TP) { const int t = c1_issue(tt + 18); if (t < 9 || dma_by_C(t) == C) mx(t); }
      const int b = blk_of(tt);
      if (b >= 0) for (int k = 0; k < 3; ++k) { const int t = w2_issue(b) + k; if (b == 0 || dma_by_C(t) == C) mx(t); }
    }
  }
  if (C) { const int b = blk_of(T); if (b >= 0 && bstart(b) == T && h_issue(b) >= 0) mx(h_issue(b) + 1); }
  return r;
}
constexpr int c1_at(int T, int n) { for (int c = 9; c < 54; ++c) if (c1_issue(c) == T) { if (n == 0) return c; --n; } return -1; }
constexpr int x_at(int T) { for (int kc = 4; kc < 9; ++kc) if (x_issue(kc) == T) return kc; return -1; }
constexpr int w2_at(int T, int k = 0) { for (int b = 0; b < NB; ++b) if (T >= w2_issue(b) && T < w2_issue(b) + 3) { if (k == 0) return b; --k; } return -1; }
constexpr int h_at(int T) { for (int b = 0; b < NB; ++b) if (h_issue(b) >= 0 && h_issue(b) == T) return b; return -1; }
constexpr int chk_at(int T) { const int b = h_at(T); return (b >= 0 && bsel(b) == 1) ? b : -1; }
constexpr int val_at(int G, int G0, int RN, int RD, int k) { for (int v = 0; v < 32; ++v) if (G0 + v * RN / RD == G) { if (k == 0) return v; --k; } return -1; }
constexpr int PRO = 9 + 8;
constexpr int waitn(int T, bool C) {
  const int r = req(T, C);
  if (r == -2) return 63;
  if (r >= 0) return after(r, T, C);
  const int need = T < 3 ? 5 * (T + 1) : PRO;
  return PRO - need + after(-1, T, C);
}
constexpr bool check() {
  if (cnt(0, true) != 0 || cnt(0, false) != 0) return false;
  for (int c = 9; c < 54; ++c) {
    if (c1_issue(c) <= c1_use(c - 9)) return false;
    if (c1_issue(c) > c1_use(c) - (c < 27 ? 1 : 2)) return false;
  }
  for (int T = 9; T < NST; ++T) { if (c1_at(T, 2) >= 0 || w2_at(T, 2) >= 0) return false; }
  for (int T = 0; T < 9; ++T) if (c1_at(T, 3) >= 0) return false;
  for (int b = 0; b < NB; ++b) {
    for (int o = 0; o < b; ++o) if (w2_buf(o) == w2_buf(b) && bstart(o) + 3 > w2_issue(b)) return false;
    if (w2_issue(b) + 2 > bstart(b) - 2) return false;
    if (w2_buf(b) < 3) for (int c = 45; c < 54; ++c) if ((c % 9) / 3 == w2_buf(b) && c1_use(c) >= w2_issue(b)) return false;
    if (h_issue(b) >= 0) {
      if (h_issue(b) + 1 > bstart(b) - 2) return false;
      for (int o = 0; o < b; ++o) if (kRing[o] == kRing[b] && bstart(o) + 3 > h_issue(b)) return false;
    }
  }
  for (int T = 0; T < NST; ++T) if (waitn(T, true) > 63 || waitn(T, false) > 63) return false;
  return true;
}
static_assert(check(), "schedule tables");
}

DI void mlp_role(const Params& p, LAS char* lds, const int i, const int s, const int jn, u32x2 epv) {
  u64 base = 0;
  using namespace sch;
  const int tid = threadIdx.x, lane = tid & 63;
  const int wave = rfl(tid >> 6), rg = wave & 3, role = wave >> 2;
  const int j = 3 * s + jn;
  const int m = lane & 31, hh = lane >> 5, blk = (lane >> 4) & 1, q = (lane & 15) >> 2, pp = lane & 3;
  const rsrc_t rx = mkrsrc(p.x, S_ * D_ * 2), rw1 = mkrsrc(p.w1, D_ * F_ * 2), rw2 = mkrsrc(p.w2, F_ * D_ * 2);
  const rsrc_t rws = mkrsrc(p.ws, WS_END);
  const unsigned ldsb = (unsigned)(size_t)lds;
  STAMP(0);
  const unsigned voX = (8 * wave + (lane >> 3)) * 1152 + (((lane & 7) ^ ((4 * (wave & 1) + (lane >> 4)) & 7)) * 16);
  const unsigned voW1 = (8 * wave + (lane >> 3)) * 9216 + (((lane & 7) ^ (((lane >> 4) & 1) << 2)) * 16);
  unsigned voW2[3];
#pragma unroll
  for (int ii = 0; ii < 3; ++ii) { const unsigned L = 1024 * (3 * wave + ii) + 16 * lane, row = L / 384, ch = (L % 384) / 16; voW2[ii] = row * 1152 + ((ch ^ ((row >> 1) & 1)) * 16); }
  const int jA = 3 * s + (jn + 1) % 3, jB = 3 * s + (jn + 2) % 3;
  const unsigned mW = ldsb + wave * 1024, mW3 = ldsb + wave * 3072, mC2 = ldsb + rg * 2048, mC6 = ldsb + rg * 6144;
  const unsigned sW1 = 768 * j, sX = 128 * i * 1152;
  const unsigned sW2[3] = {(unsigned)(384 * j * 1152 + 384 * jn), (unsigned)(384 * jA * 1152 + 384 * jn), (unsigned)(384 * jB * 1152 + 384 * jn)};
  auto dmaW1 = [&](auto Cc) { constexpr int c = decltype(Cc)::value;
    dma16i<R1 + (c % 9) * 8192, (64 * c1_kc(c)) * 9216 + 128 * c1_unit(c)>(mW, voW1, rw1, sW1); };
  auto dmaX = [&](auto Kc) { constexpr int kc = decltype(Kc)::value;
    dma16i<R2 + x_pos(kc) * 16384, kc * 128>(mW, voX, rx, sX);
    dma16i<R2 + x_pos(kc) * 16384 + 8192, 64 * 1152 + kc * 128>(mW, voX, rx, sX); };
  auto dmaW2 = [&](auto Bc, auto Kc) { constexpr int b = decltype(Bc)::value, k = decltype(Kc)::value;
    dma16i<w2_buf(b) * 24576 + k * 1024, 64 * bunit(b) * 1152>(mW3, voW2[k], rw2, sW2[bsel(b)]); };
  const unsigned voW1c = (16 * rg + (lane >> 3)) * 9216 + (((lane & 7) ^ (((lane >> 4) & 1) << 2)) * 16);
  unsigned voW2c[3];
  auto mkW2c = [&](int l) {
#pragma unroll
    for (int ii = 0; ii < 3; ++ii) { const unsigned L = 1024 * ii + 16 * l, row = L / 384, ch = (L % 384) / 16; voW2c[ii] = (16 * rg + row) * 1152 + ((ch ^ ((row >> 1) & 1)) * 16); }
  };
  auto dmaW1c = [&](auto Cc, auto Hc) { constexpr int c = decltype(Cc)::value, h = decltype(Hc)::value;
    dma16i<R1 + (c % 9) * 8192 + h * 1024, (64 * c1_kc(c) + 8 * h) * 9216 + 128 * c1_unit(c)>(mC2, voW1c, rw1, sW1); };
  auto dmaW2c = [&](auto Bc, auto Kc) { constexpr int b = decltype(Bc)::value, k = decltype(Kc)::value;
    dma16i<w2_buf(b) * 24576 + k * 1024, (64 * bunit(b) + 8 * (k / 3)) * 1152>(mC6, voW2c[k % 3], rw2, sW2[bsel(b)]); };
  auto site = [&](auto Tc, auto Sc, auto Cr) {
    constexpr int T = decltype(Tc)::value, st = decltype(Sc)::value; constexpr bool C = decltype(Cr)::value;
    if constexpr (T < 9) {
      if constexpr (st < 3 && c1_at(T, st) >= 0) dmaW1(IC<c1_at(T, st)>{});
      if constexpr (st == 3 && x_at(T) >= 0) dmaX(IC<x_at(T)>{});
    } else if constexpr (dma_by_C(T) == C) {
      if constexpr (st < 2 && c1_at(T, 0) >= 0) dmaW1c(IC<c1_at(T, 0)>{}, Sc);
      if constexpr (st < 2 && c1_at(T, 1) >= 0) dmaW1c(IC<c1_at(T, 1)>{}, Sc);
    }
    if constexpr (w2_at(T) == 0) { if constexpr (st == 3) dmaW2(IC<0>{}, IC<T - w2_issue(0)>{}); }
    else if constexpr (dma_by_C(T) == C && w2_at(T) > 0 && st >= 2) dmaW2c(IC<w2_at(T)>{}, IC<2 * (T - w2_issue(w2_at(T) < 0 ? 0 : w2_at(T))) + (st - 2)>{});
    if constexpr (dma_by_C(T) == C && w2_at(T, 1) > 0 && st < 2) dmaW2c(IC<w2_at(T, 1)>{}, IC<2 * (T - w2_issue(w2_at(T, 1) < 0 ? 0 : w2_at(T, 1))) + st>{});
  };
  auto allsites = [&](auto Tc, auto Cr) { site(Tc, IC<0>{}, Cr); site(Tc, IC<1>{}, Cr); site(Tc, IC<2>{}, Cr); site(Tc, IC<3>{}, Cr); };
  if (wave == 0) dma16(ldsb + L_B1RAW, lane * 16, mkrsrc(p.b1 + 384 * j, 768), 0);
  if (wave == 1) dma16(ldsb + L_B2RAW, lane * 16, mkrsrc(p.b2 + 192 * jn, 384), 0);
  sfor<0, 3>([&](auto Kc) { constexpr int k = decltype(Kc)::value; dmaX(Kc); dmaW1(IC<3 * k>{}); dmaW1(IC<3 * k + 1>{}); dmaW1(IC<3 * k + 2>{}); });
  dmaX(IC<3>{});

  const int sw = (q >> 1) & 1;
  const unsigned a1row = ldsb + R1 + (8 * hh + q) * 128 + 32 * blk + 8 * pp;
  const unsigned a1e = a1row + 64 * sw, a1o = a1row + 64 * (1 - sw);
  const unsigned a2row = ldsb + (4 * hh + q) * 384 + 96 * (2 * blk + (pp >> 1)) + 8 * (pp & 1);
  const unsigned a2e = a2row + 16 * sw, a2o = a2row + 16 * (1 - sw);
  const unsigned ax = ldsb + R2 + (32 * rg + m) * 128 + ((hh ^ ((m >> 1) & 7)) * 16);
  auto trr = [&](unsigned addr, unsigned d) -> bf16x8 {
    s16x4 lo = __builtin_amdgcn_ds_read_tr16_b64_v4i16((LAS s16x4*)(size_t)addr);
    s16x4 hi = __builtin_amdgcn_ds_read_tr16_b64_v4i16((LAS s16x4*)(size_t)(addr + d));
    return __builtin_bit_cast(bf16x8, __builtin_shufflevector(lo, hi, 0, 1, 2, 3, 4, 5, 6, 7));
  };
  const f32x16 zero16 = {};
  const unsigned hbase_own = WS_H + (unsigned)(i * 12 + j) * 98304u;
  const unsigned voH = rg * 4096 + lane * 16;
  float sd = 1.f, k0 = 0.f, k1 = 0.f, mean_f = 0.f;
  bf16x8 augB = {};
  u32x2 pollv = {0u, 0u};
  float st2[4] = {0.f, 0.f, 0.f, 0.f};
  f32x4 st1 = {0.f, 0.f, 0.f, 0.f};
  unsigned csv[8];
  auto actFA = [&](int l) { return l < 16 && (384 * j) / 576 + (l >> 3) <= (384 * j + 383) / 576; };
  auto voFA = [&](int l) -> unsigned { return actFA(l) ? WS_FA + (((384 * j) / 576 + (l >> 3)) * 8 + (l & 7)) * FL_STRIDE : WS_FA; };

  bf16x8 xr[36];
  f32x16 acc[2][2];
#define PRIOWIN 12
#define PFP_ 4
  constexpr int PF = 4, PFP = PFP_;
  bf16x8 pf[PF];
  unsigned b1e = a1e + role * 8192, b1o = a1o + role * 8192, b1en = 0, b1on = 0;
  asm volatile("" : "+v"(b1e), "+v"(b1o));
  auto rd1 = [&](auto Ic, auto NUc) -> bf16x8 { constexpr int I = decltype(Ic)::value, NU = decltype(NUc)::value;
    return trr(((I & 1) ? b1o : b1e) + ((I >> 1) % NU) * 8192 + (I / (2 * NU)) * 2048, 512); };
  auto rd1n = [&](auto Dc, auto Ic) -> bf16x8 { constexpr int D = decltype(Dc)::value, I = decltype(Ic)::value;
    if constexpr (D > 0) return trr(((I & 1) ? b1o : b1e) + D * 8192 + (I >> 1) * 2048, 512);
    else return trr(((I & 1) ? b1on : b1en) + (I >> 1) * 2048, 512); };
  auto adv1 = [&](auto Dc) { constexpr int D = decltype(Dc)::value; b1e += D * 8192; b1o += D * 8192; asm volatile("" : "+v"(b1e), "+v"(b1o)); };
  u32x4 hv[4];
  const unsigned aHO = ldsb + L_HO + rg * 1024 + lane * 16;
  float ez[32], eo[32];
  auto epigap = [&](auto Uc, auto Bc, auto Gc, auto G0c, auto RNc, auto RDc) {
    constexpr int uu = decltype(Uc)::value, BUF = decltype(Bc)::value, G = decltype(Gc)::value, G0 = decltype(G0c)::value, RN = decltype(RNc)::value, RD = decltype(RDc)::value;
    sfor<0, 2>([&](auto Kc) { constexpr int v = val_at(G - 3, G0, RN, RD, decltype(Kc)::value); if constexpr (v >= 0) ez[v] = __builtin_fmaf(ez[v], sd, sd); });
    sfor<0, 2>([&](auto Kc) { constexpr int v = val_at(G, G0, RN, RD, decltype(Kc)::value);
      if constexpr (v >= 0) { const float w = acc[BUF][v >> 4][v & 15], w2 = w * w, pz = __builtin_fmaf(k1, w2, k0); ez[v] = pz * w; } });
    sfor<0, 2>([&](auto Kc) { constexpr int v = val_at(G - 5, G0, RN, RD, decltype(Kc)::value);
      if constexpr (v >= 0) eo[v] = acc[BUF][v >> 4][v & 15] * ez[v]; });
    sfor<0, 2>([&](auto Kc) { constexpr int v = val_at(G - 3, G0, RN, RD, decltype(Kc)::value); if constexpr (v >= 0) ez[v] = __builtin_amdgcn_rcpf(ez[v]); });
    sfor<0, 2>([&](auto Kc) { constexpr int v = val_at(G - 2, G0, RN, RD, decltype(Kc)::value); if constexpr (v >= 0) ez[v] = __builtin_amdgcn_exp2f(ez[v]); });
    sfor<0, 2>([&](auto Kc) { constexpr int v = val_at(G - 5, G0, RN, RD, decltype(Kc)::value);
      if constexpr (v >= 0) {
        if constexpr (v & 1) hv[v >> 3][(v & 7) >> 1] = pk2(eo[v - 1], eo[v]);
        if constexpr ((v & 7) == 7) {
          if constexpr (uu == 1 || uu == 2) {
            __builtin_amdgcn_raw_buffer_store_b128(hv[v >> 3], rws, voH + (v >> 3) * 1024, hbase_own + uu * 16384, 16);
          } else *(LAS u32x4*)(size_t)(aHO + ((v >> 3) & 1) * 4096) = hv[v >> 3];
        }
      } });
  };
  auto aug = [&](auto Uc, auto Bc) {
    constexpr int uu = decltype(Uc)::value, BUF = decltype(Bc)::value;
#pragma unroll
    for (int t = 0; t < 2; ++t) {
      const bf16x8 af = *(LAS bf16x8*)(lds + L_AUG + (64 * uu + 32 * t + m) * 16);
      acc[BUF][t] = __builtin_amdgcn_mfma_f32_32x32x16_bf16(af, augB, acc[BUF][t], 0, 0, 0);
    }
  };
  auto g1stage = [&](auto Tc, auto Cc, auto Bc, auto NUc, auto Nc, auto Cr, auto&& work, auto&& syncnext) {
    constexpr int T = decltype(Tc)::value, c = decltype(Cc)::value, BUF = decltype(Bc)::value, NU = decltype(NUc)::value, NEXT = decltype(Nc)::value, kc = c1_kc(c);
    constexpr int NG = 8 * NU, PF = T < 9 ? 4 : PFP, G0 = T < 9 ? 0 : (8 * (T - 9)) % PF;
    constexpr int DN = NEXT >= 0 ? NEXT % 9 - c % 9 : 1;
    if constexpr (NEXT >= 0 && DN < 0) { b1en = b1e + DN * 8192; b1on = b1o + DN * 8192; asm volatile("" : "+v"(b1en), "+v"(b1on)); }
    __builtin_amdgcn_sched_barrier(0);
    sfor<0, NG>([&](auto Ic) {
      constexpr int I = decltype(Ic)::value, ks = I / (2 * NU), un = (I >> 1) % NU, tile = I & 1;
      const bf16x8 cur = pf[(G0 + I) % PF];
      if constexpr (I + PF < NG) pf[(G0 + I) % PF] = rd1(IC<I + PF>{}, NUc);
      else if constexpr (NEXT >= 0) pf[(G0 + I) % PF] = rd1n(IC<DN>{}, IC<I + PF - NG>{});
      acc[BUF + un][tile] = __builtin_amdgcn_mfma_f32_32x32x16_bf16(cur, xr[4 * kc + ks], (kc == 0 && ks == 0) ? zero16 : acc[BUF + un][tile], 0, 0, 0);
      __builtin_amdgcn_sched_barrier(0);
      if constexpr (I == NG - 1 && NEXT >= 0) adv1(IC<DN>{});
      if constexpr (I == 0 * NU) site(Tc, IC<0>{}, Cr);
      if constexpr (I == 2 * NU) site(Tc, IC<1>{}, Cr);
      if constexpr (I == 4 * NU) site(Tc, IC<2>{}, Cr);
      if constexpr (I == 6 * NU) site(Tc, IC<3>{}, Cr);
      if constexpr (I == NG - 1) {
        asm volatile("" : "+v"(acc[BUF][0]), "+v"(acc[BUF][1]));
        if constexpr (NU == 2) asm volatile("" : "+v"(acc[BUF + NU - 1][0]), "+v"(acc[BUF + NU - 1][1]));
        syncnext();
      }
      work(Ic, cur);
      __builtin_amdgcn_sched_barrier(0);
    });
  };
  auto dot2 = [](unsigned a, unsigned b, float c) { return __builtin_amdgcn_fdot2_f32_bf16(__builtin_bit_cast(bf16x2, a), __builtin_bit_cast(bf16x2, b), c, false); };
  const unsigned selw = ((lane & 15) == ((lane >> 4) & 1)) ? 0x3f803f80u : 0u;
  const bf16x8 selA = __builtin_bit_cast(bf16x8, u32x4{selw, selw, selw, selw});
  auto xfinish = [&]() {
    auto both = [](float v) { const unsigned u = __builtin_bit_cast(unsigned, v); const auto r = __builtin_amdgcn_permlane32_swap(u, u, false, false); return asf(r[0]) + asf(r[1]); };
    const float t2 = both((st2[0] + st2[1]) + (st2[2] + st2[3]));
    const int l = flane();
    if (l < 16) { *(LAS float*)(lds + L_ST + role * 512 + (32 * rg + l) * 4) = st1[0]; *(LAS float*)(lds + L_ST + role * 512 + (32 * rg + 16 + l) * 4) = st1[1]; }
    if (l < 32) *(LAS float*)(lds + L_ST + 1024 + role * 512 + (32 * rg + l) * 4) = t2;
    asm volatile("" : "+v"(csv[0]), "+v"(csv[1]), "+v"(csv[2]), "+v"(csv[3]), "+v"(csv[4]), "+v"(csv[5]), "+v"(csv[6]), "+v"(csv[7]));
    if (wave < 6) {
      const float cs = ((asf(csv[0]) + asf(csv[1])) + (asf(csv[2]) + asf(csv[3]))) + ((asf(csv[4]) + asf(csv[5])) + (asf(csv[6]) + asf(csv[7]))), ch = rb(cs);
      const int f = 64 * wave + l;
      *(LAS u32x4*)(lds + L_AUG + f * 16) = u32x4{pk2(ch, cs - ch), *(LAS u16*)(lds + L_B1RAW + f * 2), 0u, 0u};
    }
    lgkm0();
  };
  auto takestats = [&]() {
    const LAS float* q = (const LAS float*)(lds + L_ST) + 32 * rg + m;
    const float s1 = q[0] + q[128], s2 = q[256] + q[384];
    const float mean = s1 * (1.0f / D_), var = __builtin_fmaxf(__builtin_fmaf(-mean, mean, s2 * (1.0f / D_)), 0.f);
    mean_f = rb(mean); sd = rb(__builtin_amdgcn_sqrtf(rb(rb(var) + 1e-5f)));
    const float r = __builtin_amdgcn_rcpf(sd); k0 = -2.302208198f * r; k1 = -0.1029432397f * r * r * r;
    const u32x4 bb = {hh ? 0u : (pk2(-mean_f, -mean_f)), hh ? 0u : (pk2(sd, 0.f)), 0u, 0u};
    augB = __builtin_bit_cast(bf16x8, bb);
  };
  auto syncX = [&](auto Tc, auto Cr) {
    constexpr int T = decltype(Tc)::value; constexpr bool C = decltype(Cr)::value;
    if constexpr (T == 8) {
      if (wave == 0) { waitv<after(5, 8, C)>(); asm volatile("" : "+v"(pollv)); const int l = flane(); if (!wall(!actFA(l) || flag_ok(pollv, base, 1))) spin_flags(p.ws, rws, voFA(l), actFA(l), base, 1); }
    }
    waitv<waitn(T, C)>();
    bar();
    if constexpr (T == 0) { base = take_epoch(p.ws, epv); STAMP(1); }
    if constexpr (T == 6) { if (wave == 0) ld8_sc1(pollv, voFA(flane()), rws, 0); }
    if constexpr (T == 8) {
      const unsigned vo = (384 * j + 64 * (wave < 6 ? wave : 0) + flane()) * 4;
#pragma unroll
      for (int r = 0; r < 8; ++r) ld4_sc1(csv[r], vo, rws, WS_CS + r * F_ * 4);
    }
  };
  auto xphase = [&](auto Cr, auto&& sync9) {
    constexpr bool C = decltype(Cr)::value;
    constexpr int NU = C ? 2 : 1;
    syncX(IC<0>{}, Cr);
    sfor<0, 9>([&](auto Tc) {
      constexpr int T = decltype(Tc)::value;
      constexpr int cc = 3 * T + (C ? 1 : 0);
      if constexpr (T > 0) adv1(IC<cc % 9 - (cc - 3) % 9>{});
      sfor<0, 4>([&](auto Kc) { constexpr int ks = decltype(Kc)::value;
        xr[4 * T + ks] = *(LAS bf16x8*)(size_t)((ax ^ (ks << 5)) + x_pos(T) * 16384); });
      sfor<0, PF>([&](auto Ic) { pf[decltype(Ic)::value] = rd1(Ic, IC<NU>{}); });
      g1stage(Tc, IC<cc>{}, IC<0>{}, IC<NU>{}, IC<-1>{}, Cr, [&](auto Ic, const bf16x8&) {
        constexpr int I = decltype(Ic)::value, ks = I / (2 * NU);
        if constexpr (I % (2 * NU) == 0 && ((ks >> 1) == (C ? 1 : 0))) {
          st1 = __builtin_amdgcn_mfma_f32_16x16x32_bf16(selA, xr[4 * T + ks], st1, 0, 0, 0);
          const u32x4 x = __builtin_bit_cast(u32x4, xr[4 * T + ks]);
#pragma unroll
          for (int e = 0; e < 4; ++e) st2[e] = dot2(x[e], x[e], st2[e]);
        }
      }, [&]() { if constexpr (T < 8) syncX(IC<T + 1>{}, Cr); else sync9(); });
    });
  };
  if (role == 0) {
    u32x4 zq[3];
    auto syncP = [&](auto Tc) {
      constexpr int T = decltype(Tc)::value;
      __builtin_amdgcn_sched_barrier(0);
      if constexpr (T == 9) { waitv<cnt(8, false)>(); xfinish(); }
      waitv<(T == TZ2 ? 0 : waitn(T, false))>();
      if constexpr (T > TP && T <= TP + 5) lgkm0();
      if constexpr (T - 1 < TP && ho_written(T - 1)) asm volatile("s_waitcnt lgkmcnt(6)" ::: "memory");
      bar();
      if constexpr (T == 9) takestats();
    };
    xphase(std::false_type{}, [&]() { syncP(IC<9>{}); });
    mkW2c(flane());
    STAMP(2);
    sfor<9, NST>([&](auto Tc) {
      constexpr int T = decltype(Tc)::value;
      if constexpr (T < TP) {
        constexpr int u = 3 + (T - 9) / 9, kc = (T - 9) % 9, PU = u == 3 ? 0 : u - 1;
        constexpr int UB = u & 1, EB = UB ^ 1;
        if constexpr (T == 9) { adv1(IC<27 % 9 - 24 % 9>{}); sfor<0, PFP>([&](auto Ic) { pf[decltype(Ic)::value] = rd1(Ic, IC<1>{}); }); }
        if constexpr (kc == 0) aug(IC<PU>{}, IC<EB>{});
        g1stage(Tc, IC<T + 18>{}, IC<UB>{}, IC<1>{}, IC<(T + 1 < TP ? T + 19 : -1)>{}, std::false_type{}, [&](auto Ic, const bf16x8&) {
          epigap(IC<PU>{}, IC<EB>{}, IC<8 * kc + decltype(Ic)::value>{}, IC<1>{}, IC<2>{}, IC<1>{}); }, [&]() { syncP(IC<T + 1>{}); });
      } else {
        if constexpr (T == TP) { STAMP(3); aug(IC<5>{}, IC<1>{}); }
        allsites(Tc, std::false_type{});
        if constexpr (T == TZ1) {
          const int l = flane();
#pragma unroll
          for (int ii = 0; ii < 3; ++ii) { const int pc = 3 * rg + ii;
            ld16_sc1_s(zq[ii], (4 * pc + (l >> 4)) * 4096 + (l & 15) * 16, rws, WS_ZT + (192 * jn + 48 * s) * 4096 + (128 * i) * 2); }
#pragma unroll
          for (int ii = 0; ii < 3; ++ii) { const unsigned L = (3 * rg + ii) * 1024 + l * 16, row = L / 96, col = L - 96 * row;
            dma16(rfl(ldsb + L_XF + (3 * rg + ii) * 1024), row * 1152 + col, rx, rfl((128 * i) * 1152 + (192 * jn + 48 * s) * 2)); }
        }
        if constexpr (T == TZ2) {
          asm volatile("" : "+v"(zq[0]), "+v"(zq[1]), "+v"(zq[2]));
          const int l = flane();
#pragma unroll
          for (int ii = 0; ii < 3; ++ii) *(LAS u32x4*)(lds + L_Z + (3 * rg + ii) * 1024 + l * 16) = zq[ii];
          lgkm0();
        }
        if constexpr (T < TP + 5) sfor<0, 8>([&](auto Ic) { epigap(IC<5>{}, IC<1>{}, IC<8 * (T - TP) + decltype(Ic)::value>{}, IC<1>{}, IC<1>{}, IC<1>{}); });
        if constexpr (T + 1 < NST) syncP(IC<T + 1>{});
      }
    });
    return;
  }
  f32x16 acc2[6];
  u32x4 hring[3][4], hown[2][4];
  unsigned b2e[2], b2o[2];
  auto base2 = [&](auto Bc) { constexpr int b = decltype(Bc)::value;
    unsigned e = a2e + w2_buf(b) * 24576, o = a2o + w2_buf(b) * 24576; asm volatile("" : "+v"(e), "+v"(o)); b2e[b & 1] = e; b2o[b & 1] = o; };
  auto rd2 = [&](auto Bc, auto Gc) -> bf16x8 { constexpr int b = decltype(Bc)::value, G = decltype(Gc)::value, ks = g_ks(b, G), tn = g_tn(b, G);
    return trr(((tn & 1) ? b2o[b & 1] : b2e[b & 1]) + ks * 16 * 384 + 32 * (tn >> 1), 8 * 384); };
  const unsigned hbA = WS_H + (unsigned)(i * 12 + jA) * 98304u, hbB = WS_H + (unsigned)(i * 12 + jB) * 98304u;
  const unsigned sH[3] = {hbase_own, hbA, hbB};
  auto issueH = [&](auto Tc, auto Gc) {
    constexpr int T = decltype(Tc)::value, G = decltype(Gc)::value;
    sfor<0, 2>([&](auto Hc) {
      constexpr int half = decltype(Hc)::value, b = h_at(T - half);
      if constexpr (b >= 0) {
        constexpr int r = kRing[b], k = 2 * half + G;
        ld16i_sc1<bunit(b) * 16384 + k * 1024>(hring[r][k], voH, rws, sH[bsel(b)]);
      }
    });
  };
  u32x2 progv = {0u, 0u};
  unsigned proga = WS_PROG + (i * 12 + j) * FL_STRIDE;
  auto voPR = [&](int l) -> unsigned { return WS_PROG + (i * 12 + (l == 0 ? jA : jB)) * FL_STRIDE; };
  auto actFZ = [&](int l) { return ((192 * jn + 48 * s) / 9 + l) <= (192 * jn + 48 * s + 47) / 9; };
  auto voFZ = [&](int l) -> unsigned { return actFZ(l) ? WS_FZ + ((192 * jn + 48 * s) / 9 + l) * FL_STRIDE : WS_FZ; };
  u32x2 pollz = {0u, 0u};
  auto syncC = [&](auto Tc) {
    constexpr int T = decltype(Tc)::value, b = blk_of(T);
    __builtin_amdgcn_sched_barrier(0);
    if constexpr (T == 9) { waitv<cnt(8, true)>(); xfinish(); }
    if constexpr (chk_at(T) >= 0) {
      constexpr int bb = chk_at(T);
      if (wave == 4) { waitv<after(T - 3, T, true)>(); asm volatile("" : "+v"(pollv)); const int l = flane();
        if (!wall(l >= 2 || flag_ok(pollv, base, needOf(bunit(bb))))) spin_flags(p.ws, rws, voPR(l), l < 2, base, needOf(bunit(bb)));
        if constexpr (bb == NB - 2) { asm volatile("" : "+v"(pollz)); if (!wall(!actFZ(l) || flag_ok(pollz, base, 1))) spin_flags(p.ws, rws, voFZ(l), actFZ(l), base, 1); } }
    }
    if constexpr (pubK(T) > 0) waitv<after(T - 5, T, true)>();
    waitv<waitn(T, true)>();
    if constexpr (b >= 0 && bstart(b < 0 ? 0 : b) == T && kRing[b < 0 ? 0 : b] >= 0) { constexpr int r = kRing[b < 0 ? 0 : b]; asm volatile("" : "+v"(hring[r][0]), "+v"(hring[r][1]), "+v"(hring[r][2]), "+v"(hring[r][3])); }
    bar();
    if constexpr (pubK(T) > 0) {
      progv.x += 1;
      if (wave == 4) asm volatile("s_mov_b64 exec, 1\n\tbuffer_store_dwordx2 %0, %1, %2, 0 offen sc1\n\ts_mov_b64 exec, -1" :: "v"(progv), "v"(proga), "s"(rws) : "memory");
    }
    if constexpr (T == 9) takestats();
    if constexpr (T == PRIOWIN) __builtin_amdgcn_s_setprio(0);
    if constexpr (chk_at(T + 2) >= 0) {
      if (wave == 4) { const int l = flane(); ld8_sc1(pollv, voPR(l), rws, 0); if constexpr (chk_at(T + 2) == NB - 2) ld8_sc1(pollz, voFZ(l), rws, 0); }
    }
    constexpr int cu = cr_unit(T), ck = cr_k(T);
    if constexpr (cu >= 0) hown[ownset(cu)][ck] = *(LAS u32x4*)(size_t)(aHO + (ck & 1) * 4096);
  };
  auto chunk_store = [&](auto Tc) {
    constexpr int T = decltype(Tc)::value, cu = cr_unit(T), ck = cr_k(T);
    if constexpr (cu >= 0) __builtin_amdgcn_raw_buffer_store_b128(hown[ownset(cu)][ck], rws, voH + ck * 1024, hbase_own + cu * 16384, 16);
  };
  const unsigned pbase = WS_PART + (unsigned)(i * 3 + jn) * (16 * 12288) + rg * 3072;
  u32x4 pkeep[9];
  auto pstore = [&](auto Cc, auto Nc) {
    constexpr int c = decltype(Cc)::value, n = decltype(Nc)::value;
    const f32x16 &a = acc2[2 * c], &d = acc2[2 * c + 1];
    const bool up = n >= s;
    auto pick = [&](const f32x16& v, int e) { return up ? v[4 * (n + 1) + e] : v[4 * n + e]; };
    const u32x4 va = {pk2(pick(a, 0), pick(a, 1)), pk2(pick(a, 2), pick(a, 3)), pk2(pick(d, 0), pick(d, 1)), pk2(pick(d, 2), pick(d, 3))};
    pkeep[3 * c + n] = va;
    __builtin_amdgcn_raw_buffer_store_b128(pkeep[3 * c + n], rws, voH + c * 1024, pbase - rg * 4096 + ((n + (up ? 1 : 0)) * 4 + s) * 12288, 16);
  };
  auto gelu2 = [&](auto Tc, auto Ic) {
    constexpr int T = decltype(Tc)::value, G = 8 * (T - TG2) + decltype(Ic)::value;
    if constexpr (T >= TG2 && G <= 68) {
      epigap(IC<2>{}, IC<1>{}, IC<G>{}, IC<1>{}, IC<2>{}, IC<1>{});
      if constexpr (G >= 20 && (G - 20) % 16 == 0) hown[ownset(2)][(G - 20) / 16] = hv[(G - 20) / 16];
    }
  };
  mkW2c(lane);
  xphase(std::true_type{}, [&]() { syncC(IC<9>{}); });
  progv = u32x2{(unsigned)base, (unsigned)(base >> 32)};
  if constexpr (PRIOWIN > 9) __builtin_amdgcn_s_setprio(1);
  STAMP(2);
  sfor<9, NST>([&](auto Tc) {
    constexpr int T = decltype(Tc)::value, b = blk_of(T), part = b >= 0 ? T - bstart(b) : 0;
    if constexpr (T < 12) {
      if constexpr (T == 9) { aug(IC<1>{}, IC<0>{}); aug(IC<2>{}, IC<1>{}); }
      allsites(Tc, std::true_type{});
      sfor<0, 8>([&](auto Ic) { epigap(IC<1>{}, IC<0>{}, IC<8 * (T - 9) + decltype(Ic)::value>{}, IC<1>{}, IC<4>{}, IC<7>{}); });
      if constexpr (T == 11) {
#pragma unroll
        for (int k = 0; k < 4; ++k) hown[ownset(1)][k] = hv[k];
        base2(IC<0>{}); sfor<0, PF>([&](auto Ic) { pf[decltype(Ic)::value] = rd2(IC<0>{}, Ic); });
      }
      syncC(IC<T + 1>{});
    } else if constexpr (b < 0) {
      allsites(Tc, std::true_type{});
      issueH(Tc, IC<0>{}); issueH(Tc, IC<1>{});
      chunk_store(Tc);
      sfor<0, 8>([&](auto Ic) { gelu2(Tc, Ic); });
      if constexpr (blk_of(T + 1) >= 0) { base2(IC<blk_of(T + 1)>{}); sfor<0, PF>([&](auto Ic) { pf[decltype(Ic)::value] = rd2(IC<blk_of(T + 1)>{}, Ic); }); }
      syncC(IC<T + 1>{});
    } else {
      constexpr bool nextblk = part == 2 && blk_of(T + 1) >= 0;
      if constexpr (nextblk) base2(IC<b + 1>{});
      __builtin_amdgcn_sched_barrier(0);
      sfor<0, 8>([&](auto Ic) {
        constexpr int I = decltype(Ic)::value, G = 8 * part + I, ks = g_ks(b, G), tn = g_tn(b, G);
        const bf16x8 cur = pf[I % PF];
        if constexpr (G + PF < 24) pf[I % PF] = rd2(IC<b>{}, IC<G + PF>{});
        else if constexpr (nextblk) pf[I % PF] = rd2(IC<b + 1>{}, IC<G + PF - 24>{});
        u32x4 hb;
        if constexpr (bsel(b) == 0) hb = hown[ownset(bunit(b))][ks]; else hb = hring[kRing[b] < 0 ? 0 : kRing[b]][ks];
        acc2[tn] = __builtin_amdgcn_mfma_f32_32x32x16_bf16(cur, __builtin_bit_cast(bf16x8, hb), (b == 0 && ks == 0) ? zero16 : acc2[tn], 0, 0, 0);
        __builtin_amdgcn_sched_barrier(0);
        if constexpr (I == 0) site(Tc, IC<0>{}, std::true_type{});
        if constexpr (I == 2) site(Tc, IC<1>{}, std::true_type{});
        if constexpr (I == 1) issueH(Tc, IC<0>{});
        if constexpr (I == 3) issueH(Tc, IC<1>{});
        if constexpr (I == 4) site(Tc, IC<2>{}, std::true_type{});
        if constexpr (I == 5) chunk_store(Tc);
        if constexpr (I == 6) site(Tc, IC<3>{}, std::true_type{});
        if constexpr (b == NB - 1 && part >= 1 && I >= 2 && I <= 4) pstore(IC<part - 1>{}, IC<I - 2>{});
        if constexpr (I == 7) {
          asm volatile("" : "+v"(acc2[0]), "+v"(acc2[1]), "+v"(acc2[2]), "+v"(acc2[3]), "+v"(acc2[4]), "+v"(acc2[5]));
          if constexpr (T + 1 < NST) syncC(IC<T + 1>{});
        }
        gelu2(Tc, Ic);
        __builtin_amdgcn_sched_barrier(0);
      });
    }
  });
  asm volatile("" :: "v"(progv), "v"(proga));
  STAMP(4);
  TLDUMP();
  const int tl = flane(), tm = tl & 31, thh = tl >> 5;
  const int pos = 128 * i + 32 * rg + tm;
  pstore(IC<2>{}, IC<0>{}); pstore(IC<2>{}, IC<1>{}); pstore(IC<2>{}, IC<2>{});
  float pre[24];
  {
    float own[24];
    sfor<0, 4>([&](auto Gc) { constexpr int g = decltype(Gc)::value;
      if (g == s) {
#pragma unroll
        for (int tn = 0; tn < 6; ++tn)
#pragma unroll
          for (int e = 0; e < 4; ++e) own[4 * tn + e] = acc2[tn][4 * g + e];
      } });
    const float rstd = __builtin_amdgcn_rcpf(sd);
#pragma unroll
    for (int tn = 0; tn < 6; ++tn) {
      const int cl = 8 * tn + 4 * thh;
      const s16x4 zt = __builtin_amdgcn_ds_read_tr16_b64_v4i16((LAS s16x4*)(lds + L_Z + (cl + ((tl & 15) >> 2)) * 256 + (32 * rg + 16 * ((tl >> 4) & 1) + 4 * (tl & 3)) * 2));
      const u32x2 bv = *(LAS u32x2*)(lds + L_B2RAW + (48 * s + cl) * 2);
      const u32x2 xv = *(LAS u32x2*)(lds + L_XF + (32 * rg + tm) * 96 + cl * 2);
#pragma unroll
      for (int e = 0; e < 4; ++e) {
        const float xe = (e & 1) ? bhi(xv[e >> 1]) : blo(xv[e >> 1]);
        const float be_ = (e & 1) ? bhi(bv[e >> 1]) : blo(bv[e >> 1]);
        const float xn = rb((xe - mean_f) * rstd);
        pre[4 * tn + e] = __builtin_fmaf(xn, b2f((u16)zt[e]), xe) + (own[4 * tn + e] + be_);
      }
    }
  }
  asm volatile("s_waitcnt vmcnt(0)" :: "v"(pkeep[0]), "v"(pkeep[1]), "v"(pkeep[2]), "v"(pkeep[3]), "v"(pkeep[4]), "v"(pkeep[5]), "v"(pkeep[6]), "v"(pkeep[7]), "v"(pkeep[8]) : "memory");
  if (tl == 0) flag_store(p.ws, WS_PFL + (((i * 3 + jn) * 4 + s) * 4 + rg) * FL_STRIDE, base + 1);
  STAMP(5);
  spin_flags(p.ws, rws, WS_PFL + (((i * 3 + jn) * 4 + (tl & 3)) * 4 + rg) * FL_STRIDE, tl < 4 && tl != s, base, 1);
  STAMP(6);
  u32x4 pa[3][3];
#pragma unroll
  for (int o = 0; o < 3; ++o) { const int so = o + (o >= s ? 1 : 0);
#pragma unroll
    for (int c = 0; c < 3; ++c) ld16_sc1_s(pa[o][c], tl * 16 + c * 1024, rws, pbase + (s * 4 + so) * 12288); }
  asm volatile("s_waitcnt vmcnt(0)" : "+v"(pa[0][0]), "+v"(pa[0][1]), "+v"(pa[0][2]), "+v"(pa[1][0]), "+v"(pa[1][1]), "+v"(pa[1][2]), "+v"(pa[2][0]), "+v"(pa[2][1]), "+v"(pa[2][2]) :: "memory");
  STAMP(8);
#pragma unroll
  for (int tn = 0; tn < 6; ++tn) {
    float o[4];
#pragma unroll
    for (int e = 0; e < 4; ++e) {
      float sum = pre[4 * tn + e];
#pragma unroll
      for (int oo = 0; oo < 3; ++oo) { const unsigned wd = pa[oo][tn >> 1][2 * (tn & 1) + (e >> 1)]; sum += (e & 1) ? bhi(wd) : blo(wd); }
      o[e] = sum;
    }
    const u32x2 ov = {pk2(o[0], o[1]), pk2(o[2], o[3])};
    *(GAS u32x2*)(p.out + (size_t)pos * D_ + 192 * jn + 48 * s + 8 * tn + 4 * thh) = ov;
  }
  STAMP(7);
}

constexpr unsigned A_CH = 12928;
constexpr unsigned A_H0 = 32, A_H1 = A_H0 + 4096 + 64, A_X = A_H1 + 4096 + 512  ;
static_assert(A_X + 4096 <= A_CH && A_X % 16 == 0, "channel image");
typedef unsigned u32x4a4 __attribute__((ext_vector_type(4), aligned(4)));
typedef float f32x4v __attribute__((ext_vector_type(4)));

constexpr unsigned A_STG = 9 * A_CH;
constexpr unsigned A_CSP = A_STG;
constexpr unsigned A_RK = 0, A_RV = 40960;
constexpr unsigned A_JUNK = A_STG + 288 * 144;
static_assert(A_JUNK + 4096 <= 163840, "aux LDS");

DI void aux_role(const Params& p, LAS char* lds, const int a, const int al, const int cset, u32x2 epv, unsigned junk0, unsigned junk1) {
  u64 base;
  const int tid = threadIdx.x, lane = tid & 63, wave = rfl(tid >> 6);
  const rsrc_t rws = mkrsrc(p.ws, WS_END);
  const unsigned ldsb = (unsigned)(size_t)lds;
  STAMP(0);
  constexpr unsigned OOB = 0x7fffff00u;
  const rsrc_t rw1 = mkrsrc(p.w1, D_ * F_ * 2);
  u32x4 wv[11];
  const int r7 = tid / 72, c72 = tid - 72 * r7;
#pragma unroll
  for (int k = 0; k < 11; ++k) {
    const int rl = r7 + 7 * k;
    wv[k] = __builtin_amdgcn_raw_buffer_load_b128(rw1, (r7 < 7 && rl < 72) ? (unsigned)((72 * al + rl) * 9216 + (576 * cset) * 2 + c72 * 16) : OOB, 0, 0);
  }
  const int pt = a >> 1, hf = a & 1;
  const rsrc_t rk = mkrsrc(p.k, S_ * D_ * 2), rv = mkrsrc(p.v, S_ * D_ * 2), rs = mkrsrc(p.shk, D_ * 8), rh = mkrsrc(p.ssk, D_ * S_ * 2);
#pragma unroll
  for (int n = 0; n < 5; ++n) {
    const unsigned L = (wave + 8 * n) * 1024 + lane * 16, row = L / 576, col = L - 576 * row;
    const int sr = 64 * pt - 3 + (int)row;
    dma16(ldsb + A_RK + rfl(wave + 8 * n) * 1024, (row < 67 && sr >= 0) ? (unsigned)(sr * 1152) + col : OOB, rk, 576 * hf);
    dma16(ldsb + A_RV + rfl(wave + 8 * n) * 1024, row < 64 ? (unsigned)((64 * pt + row) * 1152) + col : OOB, rv, 576 * hf);
  }
  u32x4 sh[3];
#pragma unroll
  for (int n = 0; n < 3; ++n) { const int it = tid + 512 * n, cp = it % 144; sh[n] = __builtin_amdgcn_raw_buffer_load_b128(rs, (288 * hf + 2 * cp) * 8, 0, 0); }
  {
    float cs[8] = {0.f, 0.f, 0.f, 0.f, 0.f, 0.f, 0.f, 0.f};
#pragma unroll
    for (int k = 0; k < 11; ++k)
#pragma unroll
      for (int e = 0; e < 4; ++e) { cs[2 * e] += blo(wv[k][e]); cs[2 * e + 1] += bhi(wv[k][e]); }
    if (r7 < 7) { LAS char* dst = lds + A_CSP + (r7 * 576 + c72 * 8) * 4;
      *(LAS f32x4*)dst = f32x4{cs[0], cs[1], cs[2], cs[3]}; *(LAS f32x4*)(dst + 16) = f32x4{cs[4], cs[5], cs[6], cs[7]}; }
    asm volatile("" :: "v"(junk0), "v"(junk1));
    base = take_epoch(p.ws, epv);
    sbar();
    if (tid < 144) {
      f32x4 t = *(LAS f32x4*)(lds + A_CSP + tid * 16);
#pragma unroll
      for (int r = 1; r < 7; ++r) { const f32x4 u = *(LAS f32x4*)(lds + A_CSP + r * 2304 + tid * 16); t = f32x4{t[0] + u[0], t[1] + u[1], t[2] + u[2], t[3] + u[3]}; }
      __builtin_amdgcn_raw_buffer_store_b128(__builtin_bit_cast(u32x4, t), rws, WS_CS + (al * F_ + 576 * cset) * 4 + tid * 16, 0, 16);
    }
  }
  STAMP(6);
  asm volatile("s_waitcnt vmcnt(0) lgkmcnt(0)\n\ts_barrier" : "+v"(sh[0]), "+v"(sh[1]), "+v"(sh[2]) :: "memory");
  if (tid == 0) flag_store(p.ws, WS_FA + (cset * 8 + al) * FL_STRIDE, base + 1);
  STAMP(2);
  unsigned touch[2];
  {
    const rsrc_t rw2 = mkrsrc(p.w2, F_ * D_ * 2);
#pragma unroll
    for (int k = 0; k < 2; ++k) { const int ln = tid + 512 * k;
      asm volatile("buffer_load_dword %0, %1, %2, %3 offen" : "=v"(touch[k]) : "v"(ln < 648 ? (unsigned)(ln * 128) : OOB), "s"(rw2), "s"((unsigned)((576 * cset + 72 * al) * 1152)) : "memory"); }
  }
  u32x4 kvo[3][2];
#pragma unroll
  for (int n = 0; n < 3; ++n) {
    const int it = (tid + 512 * n) < 1152 ? tid + 512 * n : tid, o = it / 144, cp = it - 144 * o;
    unsigned kk[11], vv[8];
#pragma unroll
    for (int r = 0; r < 11; ++r) kk[r] = *(LAS unsigned*)(lds + A_RK + (8 * o + r) * 576 + cp * 4);
#pragma unroll
    for (int r = 0; r < 8; ++r) vv[r] = *(LAS unsigned*)(lds + A_RV + (8 * o + r) * 576 + cp * 4);
    float o0[8], o1[8];
#pragma unroll
    for (int r = 0; r < 8; ++r) {
      float a0 = blo(kk[r]) * bhi(sh[n][1]); a0 = __builtin_fmaf(blo(kk[r + 1]), blo(sh[n][1]), a0); a0 = __builtin_fmaf(blo(kk[r + 2]), bhi(sh[n][0]), a0); a0 = __builtin_fmaf(blo(kk[r + 3]), blo(sh[n][0]), a0);
      float a1 = bhi(kk[r]) * bhi(sh[n][3]); a1 = __builtin_fmaf(bhi(kk[r + 1]), blo(sh[n][3]), a1); a1 = __builtin_fmaf(bhi(kk[r + 2]), bhi(sh[n][2]), a1); a1 = __builtin_fmaf(bhi(kk[r + 3]), blo(sh[n][2]), a1);
      o0[r] = a0 * blo(vv[r]); o1[r] = a1 * bhi(vv[r]);
    }
    kvo[n][0] = u32x4{pk2(o0[7], o0[6]), pk2(o0[5], o0[4]), pk2(o0[3], o0[2]), pk2(o0[1], o0[0])};
    kvo[n][1] = u32x4{pk2(o1[7], o1[6]), pk2(o1[5], o1[4]), pk2(o1[3], o1[2]), pk2(o1[1], o1[0])};
  }
#pragma unroll
  for (int n = 0; n < 3; ++n) {
    const int it = tid + 512 * n, o = it / 144, cp = it - 144 * o;
    if (it < 1152) { LAS char* dst = lds + A_STG + (2 * cp) * 144 + o * 16; *(LAS u32x4*)dst = kvo[n][0]; *(LAS u32x4*)(dst + 144) = kvo[n][1]; }
  }
  STAMP(8);
  u32x4 hA[5]; unsigned hB[5];
#pragma unroll
  for (int n = 0; n < 5; ++n) {
    const int un = wave + 8 * n, c = un >> 2, i = un & 3;
    const unsigned go = un < 36 ? (unsigned)((9 * a + c) * 4096 + i * 1024 + lane * 16) : OOB;
    asm volatile("buffer_load_dwordx4 %0, %2, %4, 0 offen\n\tbuffer_load_dword %1, %3, %4, 0 offen" : "=&v"(hA[n]), "=&v"(hB[n]) : "v"(go), "v"(un < 36 ? go + 16 : OOB), "s"(rh) : "memory");
  }
  sbar();
  if (tid < 9 * 35) { const int c = tid / 35, i = tid - 35 * c;
    const unsigned off = c * A_CH + (i < 2 ? i * 16 : i < 3 ? A_H1 - 32 : A_X - 512 + (i - 3) * 16);
    *(LAS u32x4*)(lds + off) = u32x4{0u, 0u, 0u, 0u}; }
#pragma unroll
  for (int n = 0; n < 5; ++n) {
    const int gch = wave + 8 * n;
    if (gch < 36) {
      const int ch = 8 * gch + (lane >> 3), o = lane & 7;
      const u32x4 v = *(LAS u32x4*)(lds + A_STG + ch * 144 + o * 16);
      __builtin_amdgcn_raw_buffer_store_b128(v, rws, WS_KVT + (288 * hf + ch) * 4096 + (64 * pt + 8 * o) * 2, 0, 16);
    }
  }
  asm volatile("s_waitcnt vmcnt(4)" : "+v"(hA[0]), "+v"(hA[1]), "+v"(hA[2]), "+v"(hA[3]), "+v"(hA[4]), "+v"(hB[0]), "+v"(hB[1]), "+v"(hB[2]), "+v"(hB[3]), "+v"(hB[4]) :: "memory");
  asm volatile("" : "+v"(touch[0]), "+v"(touch[1]));
#pragma unroll
  for (int n = 0; n < 5; ++n) {
    const int un = wave + 8 * n, c = un >> 2, i = un & 3;
    if (un < 36) {
      const u32x4 A = hA[n];
      *(LAS u32x4*)(lds + c * A_CH + A_H0 + i * 1024 + lane * 16) = A;
      *(LAS u32x4*)(lds + c * A_CH + A_H1 + i * 1024 + lane * 16) =
          u32x4{__builtin_amdgcn_alignbit(A[1], A[0], 16), __builtin_amdgcn_alignbit(A[2], A[1], 16), __builtin_amdgcn_alignbit(A[3], A[2], 16), __builtin_amdgcn_alignbit(hB[n], A[3], 16)};
      if (i == 0 && lane == 0) *(LAS u32x4*)(lds + c * A_CH + A_H1 - 16) = u32x4{0u, 0u, 0u, A[0] << 16};
    }
  }
  STAMP(5);
  publish_flag(p.ws, WS_FB + a * FL_STRIDE, base + 1);
  STAMP(1);
  {
    const int hfc = (9 * a) >= 288 ? 1 : 0;
    if (wave == 0) {
      spin_flags(p.ws, rws, WS_FB + (2 * (lane & 31) + hfc) * FL_STRIDE, lane < 32, base, 1);
      __builtin_amdgcn_fence(__ATOMIC_ACQUIRE, "agent");
      asm volatile("s_waitcnt vmcnt(0)" ::: "memory");
    }
    sbar();
#pragma unroll
    for (int n = 0; n < 5; ++n) {
      const int un = wave + 8 * n, c = un >> 2, i = un & 3;
      if (un < 36) dma16(rfl(ldsb + c * A_CH + A_X + i * 1024), lane * 16, rws, rfl(WS_KVT + (9 * a + c) * 4096 + i * 1024));
    }
    asm volatile("s_waitcnt vmcnt(0) lgkmcnt(0)\n\ts_barrier" ::: "memory");
  }
  STAMP(3);
  const int b = lane & 15, g = lane >> 4;
  const int odd = (b + 1) & 1;
  const unsigned aH = (odd ? A_H1 : A_H0) + 2 * (9 + b - 8 * g - odd);
  const unsigned aX = A_X + 32 * b - 32 + 16 * g;
  auto rdH = [&](unsigned chb, int mp) -> bf16x8 { return __builtin_bit_cast(bf16x8, (u32x4)*(LAS u32x4a4*)(lds + chb + aH + 64 * mp)); };
  auto rdX = [&](unsigned chb, int n, int kappa) -> bf16x8 { return *(LAS bf16x8*)(lds + chb + aX + 512 * n - 64 * kappa); };
  const float dd0 = b2f(p.ssd[9 * a + wave]), dd8 = b2f(p.ssd[9 * a + 8]);
  auto finish = [&](int c, int t, f32x4v y) {
    const u32x2 kvr = *(LAS u32x2*)(lds + c * A_CH + A_X + (32 * t + 2 * b + (g >> 1)) * 16 + ((g & 1) ? 0 : 8));
    const float dd = c == 8 ? dd8 : dd0;
    const float z0 = rb(y[0]) + rb(bhi(kvr[1]) * dd), z1 = rb(y[1]) + rb(blo(kvr[1]) * dd), z2 = rb(y[2]) + rb(bhi(kvr[0]) * dd), z3 = rb(y[3]) + rb(blo(kvr[0]) * dd);
    __builtin_amdgcn_raw_buffer_store_b64(u32x2{pk2(z0, z1), pk2(z2, z3)}, rws, WS_ZT + (9 * a + c) * 4096 + (256 * t + 16 * b + 4 * g) * 2, 0, 16);
  };
  {
    f32x4v acc[8];
#pragma unroll
    for (int t = 0; t < 8; ++t) acc[t] = f32x4v{0.f, 0.f, 0.f, 0.f};
    unsigned bH[4], bX = ldsb + wave * A_CH + aX;
#pragma unroll
    for (int q4 = 0; q4 < 4; ++q4) bH[q4] = ldsb + wave * A_CH + aH + 1024 * q4;
    auto ldH = [&](int m) -> bf16x8 { return __builtin_bit_cast(bf16x8, (u32x4)*(LAS u32x4a4*)(size_t)(bH[m >> 1] + 512 * (m & 1))); };
    auto ldX = [&](int n) -> bf16x8 { return *(LAS bf16x8*)(size_t)(bX + 512 * n); };
    auto step = [&]() {
#pragma unroll
      for (int q4 = 0; q4 < 4; ++q4) bH[q4] += 64;
      bX -= 64;
      asm volatile("" : "+v"(bH[0]), "+v"(bH[1]), "+v"(bH[2]), "+v"(bH[3]), "+v"(bX));
    };
    bf16x8 H[2][8], X[2][8];
    asm volatile("" : "+v"(bH[0]), "+v"(bH[1]), "+v"(bH[2]), "+v"(bH[3]), "+v"(bX));
#pragma unroll
    for (int m = 0; m < 8; ++m) { H[0][m] = ldH(m); X[0][m] = ldX(m); }
#pragma unroll 1
    for (int kp = 0; kp < 4; ++kp) {
#pragma unroll
      for (int hb = 0; hb < 2; ++hb) {
        step();
        __builtin_amdgcn_sched_barrier(0);
#pragma unroll
        for (int m = 0; m < 8; ++m) {
          H[hb ^ 1][m] = ldH(m); X[hb ^ 1][m] = ldX(m);
#pragma unroll
          for (int t = m; t < 8; ++t) acc[t] = __builtin_amdgcn_mfma_f32_16x16x32_bf16(H[hb][m], X[hb][t - m], acc[t], 0, 0, 0);
          __builtin_amdgcn_sched_barrier(0);
        }
      }
    }
#pragma unroll
    for (int t = 0; t < 8; ++t) finish(wave, t, acc[t]);
  }
  STAMP(9);
  {
    const unsigned chb = 8 * A_CH;
    f32x4v acc0 = {0.f, 0.f, 0.f, 0.f}, acc1 = acc0;
    const int t = wave;
    bf16x8 H[2][4], X[2][4];
    auto rd4 = [&](int hb, int g4) {
#pragma unroll
      for (int k = 0; k < 4; ++k) { H[hb][k] = rdH(chb + 256 * g4, k); X[hb][k] = *(LAS bf16x8*)(lds + chb + aX + 512 * t - 256 * g4 - 64 * k); }
    };
    rd4(0, 0);
#pragma unroll 1
    for (int g8 = 0; g8 <= t; ++g8) {
      rd4(1, 2 * g8 + 1);
      __builtin_amdgcn_sched_barrier(0);
#pragma unroll
      for (int k = 0; k < 4; k += 2) { acc0 = __builtin_amdgcn_mfma_f32_16x16x32_bf16(H[0][k], X[0][k], acc0, 0, 0, 0); acc1 = __builtin_amdgcn_mfma_f32_16x16x32_bf16(H[0][k + 1], X[0][k + 1], acc1, 0, 0, 0); }
      __builtin_amdgcn_sched_barrier(0);
      rd4(0, g8 < t ? 2 * g8 + 2 : 0);
      __builtin_amdgcn_sched_barrier(0);
#pragma unroll
      for (int k = 0; k < 4; k += 2) { acc0 = __builtin_amdgcn_mfma_f32_16x16x32_bf16(H[1][k], X[1][k], acc0, 0, 0, 0); acc1 = __builtin_amdgcn_mfma_f32_16x16x32_bf16(H[1][k + 1], X[1][k + 1], acc1, 0, 0, 0); }
      __builtin_amdgcn_sched_barrier(0);
    }
    finish(8, t, acc0 + acc1);
  }
  publish_flag(p.ws, WS_FZ + a * FL_STRIDE, base + 1);
  STAMP(4);
  if (a == 63 && wave == 0) {
#pragma unroll 1
    for (int k = 0; k < 4; ++k) spin_flags(p.ws, rws, WS_ARR + (64 * k + lane) * FL_STRIDE, true, base, 0);
    if (lane == 0) flag_store(p.ws, WS_EPOCH, base >> 4);
  }
}

#define CODE_BYTES 79872u
__global__ __launch_bounds__(512, 2) void k_main(Params p) {
  extern __shared__ __attribute__((aligned(16))) char smem[];
  LAS char* lds = (LAS char*)smem;
  const int b = blockIdx.x, g = b & 7, qq = b >> 3;
  u32x2 epv;
  asm volatile("global_load_dwordx2 %0, %1, %2 sc1" : "=v"(epv) : "v"(0u), "s"(p.ws) : "memory");
  if (qq < 24) mlp_role(p, lds, 8 * (g & 1) + qq / 3, g >> 1, qq % 3, epv);
  else {
    u64 pc; asm volatile("s_getpc_b64 %0" : "=s"(pc));
    unsigned junk0, junk1;
    { const unsigned l = threadIdx.x & 63, n = CODE_BYTES / 128 / 8, o0 = ((qq - 24) * n + (l < n ? l : 0)) * 128, o1 = ((qq - 24) * n + (l + 64 < n ? l + 64 : 0)) * 128;
      if (threadIdx.x < 64) asm volatile("global_load_dword %0, %2, %4\n\tglobal_load_dword %1, %3, %4" : "=&v"(junk0), "=&v"(junk1) : "v"(o0), "v"(o1), "s"(pc & ~(u64)255) : "memory");
      else junk0 = junk1 = 0; }
    aux_role(p, lds, (qq - 24) * 8 + g, qq - 24, g, epv, junk0, junk1);
  }
}
}
#if defined(__HIP_DEVICE_COMPILE__)
#pragma clang attribute pop
#endif

extern "C" void kernel_launch(void* const* d_in, const int* in_sizes, int n_in, void* d_out, int out_size, void* d_ws, size_t ws_size, hipStream_t stream) {
  using namespace mk;
  Params p;
  p.x = (const u16*)d_in[0]; p.k = (const u16*)d_in[1]; p.v = (const u16*)d_in[2]; p.shk = (const u16*)d_in[3]; p.ssk = (const u16*)d_in[4]; p.ssd = (const u16*)d_in[5];
  p.w1 = (const u16*)d_in[7]; p.b1 = (const u16*)d_in[8]; p.w2 = (const u16*)d_in[9]; p.b2 = (const u16*)d_in[10];
  p.out = (u16*)d_out; p.ws = (char*)d_ws;
  (void)hipFuncSetAttribute((const void*)k_main, hipFuncAttributeMaxDynamicSharedMemorySize, LDS_BYTES);
  k_main<<<256, 512, LDS_BYTES, stream>>>(p);
}
```
